```python
import torch

def _softmax(x, axis=-1):
    return torch.softmax(x, dim=axis)
def _gelu(x):
    return torch.nn.functional.gelu(x, approximate="tanh")
def _silu(x):
    return torch.nn.functional.silu(x)
def _sigmoid(x):
    return torch.sigmoid(x)
def _layernorm(x, axis=-1, eps=1e-5):
    m = x.mean(dim=axis, keepdim=True)
    v = x.var(dim=axis, keepdim=True, unbiased=False)
    return (x - m) / torch.sqrt(v + eps)
def _scatter_add(tgt, idx, src, axis=0):
    return tgt.clone().index_add_(axis, idx, src)
def _segment_sum(src, seg, n):
    out = torch.zeros((n,) + src.shape[1:], dtype=src.dtype)
    return out.index_add_(0, seg, src)
def _pad(x, pw):
    flat = tuple(v for pair in reversed(pw) for v in pair)
    return torch.nn.functional.pad(x, flat)

kernel_name = 'gated_mix__sliding_block_cached__geglu_ffn__decode'

def setup_inputs():

    _w = (lambda a: (lambda t: t.to(torch.bfloat16) if t.is_floating_point() else t)(torch.as_tensor(a)))
    def _A_setup():
        B, Sq, D = 1, 1, 640
        Hq, Hkv, Sk, Dh = 32, 32, 32768, 192
        assert Hq % Hkv == 0, (Hq, Hkv)
        return {
            "x": _w((lambda *s: torch.randn(*s, dtype=torch.bfloat16) * 0.05)(B, Sq, D)),
            "k_cache": _w((lambda *s: torch.randn(*s, dtype=torch.bfloat16) * 6.4)(B, Hkv, Sk, Dh)),
            "v_cache": _w((lambda *s: torch.randn(*s, dtype=torch.bfloat16) * 0.2)(B, Hkv, Sk, Dh)),
            "wq": _w((lambda *s: torch.randn(*s, dtype=torch.bfloat16) * 6.4)(D, Hq * Dh) * (D ** -0.5)),
            "wo": _w((lambda *s: torch.randn(*s, dtype=torch.bfloat16) * 0.2)(Hq * Dh, D) * ((Hq * Dh) ** -0.5)),
        }
    _A_in = _A_setup()

    def _B_setup():
        B, S, D, F = 1, 1, 640, 1536
        return {
            "x": _w((lambda *s: torch.randn(*s, dtype=torch.bfloat16) * 0.05)(B, S, D)),
            "w_gate": _w((lambda *s: torch.randn(*s, dtype=torch.bfloat16) * 0.05)(D, F)),
            "w_up": _w((lambda *s: torch.randn(*s, dtype=torch.bfloat16) * 0.05)(D, F)),
            "w_down": _w((lambda *s: torch.randn(*s, dtype=torch.bfloat16) * 0.05)(F, D)),
        }
    _B_in = _B_setup()

    return {
        "A_x": _A_in["x"],
        "A_k_cache": _A_in["k_cache"],
        "A_v_cache": _A_in["v_cache"],
        "A_wq": _A_in["wq"],
        "A_wo": _A_in["wo"],
        "B_x": _B_in["x"],
        "B_w_gate": _B_in["w_gate"],
        "B_w_up": _B_in["w_up"],
        "B_w_down": _B_in["w_down"],
        "gate_w": _w((lambda *s: torch.randn(*s, dtype=torch.bfloat16) * 0.1)(640) * 16.0),
    }

def reference(A_x, A_k_cache, A_v_cache, A_wq, A_wo, B_x, B_w_gate, B_w_up, B_w_down, gate_w):
    def _A_ref(x, k_cache, v_cache, wq, wo):
        B, Sq, D = x.shape
        Hq, Dh = 32, 192
        Hkv, Sk = k_cache.shape[1], k_cache.shape[2]
        W = min(4096, max(256, Sk // 4))
        rep = Hq // Hkv
        q = (lambda x, *d: x.permute(*d))((x @ wq).reshape(B, Sq, Hq, Dh), 0, 2, 1, 3)
        k = (lambda x, n, axis: torch.repeat_interleave(x, n, dim=axis))(k_cache[:, :, Sk - W:, :], rep, axis=1)
        v = (lambda x, n, axis: torch.repeat_interleave(x, n, dim=axis))(v_cache[:, :, Sk - W:, :], rep, axis=1)
        scores = torch.einsum("bhqd,bhkd->bhqk", q, k) * (Dh ** -0.5)
        p = _softmax(scores, axis=-1)
        o = torch.einsum("bhqk,bhkd->bhqd", p, v)
        return (lambda x, *d: x.permute(*d))(o, 0, 2, 1, 3).reshape(B, Sq, Hq * Dh) @ wo

    def _B_ref(x, w_gate, w_up, w_down):
        return (_gelu(x @ w_gate) * (x @ w_up)) @ w_down

    x = A_x
    g = _sigmoid((x * gate_w).sum(-1, keepdims=True))
    a = _A_ref(x, A_k_cache, A_v_cache, A_wq, A_wo)
    b = _B_ref(x, B_w_gate, B_w_up, B_w_down)
    return g * a + (1 - g) * b
```

```cpp
#include <hip/hip_runtime.h>
#include <hip/hip_bf16.h>
#include <stdint.h>

typedef unsigned u32x4 __attribute__((ext_vector_type(4)));
typedef __bf16 bf16x2 __attribute__((ext_vector_type(2)));
constexpr int D = 640, H = 32, SK = 32768, DH = 192, W = 4096, F = 1536;

__device__ __forceinline__ float bflo(unsigned u) { return __uint_as_float(u << 16); }
__device__ __forceinline__ float bfhi(unsigned u) { return __uint_as_float(u & 0xffff0000u); }
__device__ __forceinline__ float bf2f(unsigned short u) { return __uint_as_float(((unsigned)u) << 16); }
__device__ __forceinline__ float rbf(float f) { return (float)(__bf16)f; }

__global__ void __launch_bounds__(256) k1(const unsigned short* x, const unsigned short* wq, const unsigned short* wg, const unsigned short* wu, float* q, float* hid) {
  __shared__ float red[2][4][64];
  int b = blockIdx.x, t = threadIdx.x, c = t & 63, w = t >> 6;
  if (b < 96) {
    int col = b * 64 + c; float a = 0.f;
    for (int r = w; r < D; r += 4) a += bf2f(x[r]) * bf2f(wq[(size_t)r * (H * DH) + col]);
    red[0][w][c] = a; __syncthreads();
    if (w == 0) q[col] = rbf(red[0][0][c] + red[0][1][c] + red[0][2][c] + red[0][3][c]);
  } else {
    int col = (b - 96) * 64 + c; float a = 0.f, u = 0.f;
    for (int r = w; r < D; r += 4) { float xv = bf2f(x[r]); a += xv * bf2f(wg[(size_t)r * F + col]); u += xv * bf2f(wu[(size_t)r * F + col]); }
    red[0][w][c] = a; red[1][w][c] = u; __syncthreads();
    if (w == 0) {
      float g = red[0][0][c] + red[0][1][c] + red[0][2][c] + red[0][3][c];
      float uu = red[1][0][c] + red[1][1][c] + red[1][2][c] + red[1][3][c];
      float ge = 0.5f * g * (1.f + tanhf(0.7978845608028654f * (g + 0.044715f * g * g * g)));
      hid[col] = ge * uu;
    }
  }
}
__global__ void __launch_bounds__(512) k2(const unsigned short* kc, const unsigned short* vc, const float* q, float* part) {
  __shared__ float sm[8][200];
  int b = blockIdx.x, h = b >> 3, s = b & 7, t = threadIdx.x, l = t & 63, w = t >> 6;
  size_t base = ((size_t)h * SK + (SK - W) + s * 512 + w * 64 + (l >> 3)) * DH + (l & 7) * 8;
  float qf[24];
  for (int c = 0; c < 3; ++c) for (int j = 0; j < 8; ++j) qf[c * 8 + j] = q[h * DH + c * 64 + (l & 7) * 8 + j];
  float sc[8];
#pragma unroll
  for (int g = 0; g < 8; ++g) {
    float a = 0.f;
#pragma unroll
    for (int c = 0; c < 3; ++c) {
      u32x4 kv = __builtin_nontemporal_load((const u32x4*)(kc + base + g * 8 * DH + c * 64));
#pragma unroll
      for (int j = 0; j < 4; ++j) a += bflo(kv[j]) * qf[c * 8 + 2 * j] + bfhi(kv[j]) * qf[c * 8 + 2 * j + 1];
    }
    a += __shfl_xor(a, 1); a += __shfl_xor(a, 2); a += __shfl_xor(a, 4);
    sc[g] = a * 0.07216878364870322f;
  }
  float m = sc[0];
#pragma unroll
  for (int g = 1; g < 8; ++g) m = fmaxf(m, sc[g]);
  m = fmaxf(m, __shfl_xor(m, 8)); m = fmaxf(m, __shfl_xor(m, 16)); m = fmaxf(m, __shfl_xor(m, 32));
  float lsum = 0.f; float acc[24];
#pragma unroll
  for (int j = 0; j < 24; ++j) acc[j] = 0.f;
#pragma unroll
  for (int g = 0; g < 8; ++g) {
    float p = __expf(sc[g] - m); lsum += p;
#pragma unroll
    for (int c = 0; c < 3; ++c) {
      u32x4 vv = __builtin_nontemporal_load((const u32x4*)(vc + base + g * 8 * DH + c * 64));
#pragma unroll
      for (int j = 0; j < 4; ++j) { acc[c * 8 + 2 * j] += p * bflo(vv[j]); acc[c * 8 + 2 * j + 1] += p * bfhi(vv[j]); }
    }
  }
  lsum += __shfl_xor(lsum, 8); lsum += __shfl_xor(lsum, 16); lsum += __shfl_xor(lsum, 32);
#pragma unroll
  for (int j = 0; j < 24; ++j) { acc[j] += __shfl_xor(acc[j], 8); acc[j] += __shfl_xor(acc[j], 16); acc[j] += __shfl_xor(acc[j], 32); }
  if (l < 8) { for (int c = 0; c < 3; ++c) for (int j = 0; j < 8; ++j) sm[w][c * 64 + l * 8 + j] = acc[c * 8 + j]; }
  if (l == 0) { sm[w][192] = m; sm[w][193] = lsum; }
  __syncthreads();
  if (t < 194) {
    float M = sm[0][192]; for (int i = 1; i < 8; ++i) M = fmaxf(M, sm[i][192]);
    float r = 0.f;
    if (t == 192) r = M; else { for (int i = 0; i < 8; ++i) r += __expf(sm[i][192] - M) * sm[i][t]; }
    part[b * 200 + t] = r;
  }
}
__global__ void __launch_bounds__(640) k3(const float* part, const float* hid, const unsigned short* wo, const unsigned short* wd, float* part2) {
  __shared__ float z[192];
  int b = blockIdx.x, t = threadIdx.x;
  const unsigned short* wm;
  if (b < 32) {
    if (t < 192) {
      float M = part[b * 8 * 200 + 192]; for (int i = 1; i < 8; ++i) M = fmaxf(M, part[(b * 8 + i) * 200 + 192]);
      float L = 0.f, o = 0.f;
      for (int i = 0; i < 8; ++i) { float e = __expf(part[(b * 8 + i) * 200 + 192] - M); L += e * part[(b * 8 + i) * 200 + 193]; o += e * part[(b * 8 + i) * 200 + t]; }
      z[t] = o / L;
    }
    wm = wo + (size_t)b * 192 * D;
  } else { if (t < 192) z[t] = hid[(b - 32) * 192 + t]; wm = wd + (size_t)(b - 32) * 192 * D; }
  __syncthreads();
  float a = 0.f;
  for (int r = 0; r < 192; ++r) a += z[r] * bf2f(wm[r * D + t]);
  part2[b * D + t] = a;
}
__global__ void __launch_bounds__(640) k4(const float* part2, const unsigned short* x, const unsigned short* gw, __hip_bfloat16* out) {
  __shared__ float red[10];
  int t = threadIdx.x;
  float v = bf2f(x[t]) * bf2f(gw[t]);
  for (int o = 32; o; o >>= 1) v += __shfl_xor(v, o);
  if ((t & 63) == 0) red[t >> 6] = v;
  __syncthreads();
  float s = 0.f; for (int i = 0; i < 10; ++i) s += red[i];
  float g = 1.f / (1.f + __expf(-s));
  float a = 0.f, bb = 0.f;
  for (int i = 0; i < 32; ++i) a += part2[i * D + t];
  for (int i = 32; i < 40; ++i) bb += part2[i * D + t];
  out[t] = (__hip_bfloat16)(g * a + (1.f - g) * bb);
}

extern "C" void kernel_launch(void* const* d_in, const int* in_sizes, int n_in, void* d_out, int out_size, void* d_ws, size_t ws_size, hipStream_t stream) {
  const unsigned short* x = (const unsigned short*)d_in[0];
  const unsigned short* kc = (const unsigned short*)d_in[1];
  const unsigned short* vc = (const unsigned short*)d_in[2];
  const unsigned short* wq = (const unsigned short*)d_in[3];
  const unsigned short* wo = (const unsigned short*)d_in[4];
  const unsigned short* wg = (const unsigned short*)d_in[6];
  const unsigned short* wu = (const unsigned short*)d_in[7];
  const unsigned short* wd = (const unsigned short*)d_in[8];
  const unsigned short* gw = (const unsigned short*)d_in[9];
  float* ws = (float*)d_ws;
  float* q = ws; float* hid = ws + 8192; float* part = ws + 16384; float* part2 = ws + 16384 + 256 * 200;
  k1<<<96 + 24, 256, 0, stream>>>(x, wq, wg, wu, q, hid);
  k2<<<256, 512, 0, stream>>>(kc, vc, q, part);
  k3<<<40, 640, 0, stream>>>(part, hid, wo, wd, part2);
  k4<<<1, 640, 0, stream>>>(part2, x, gw, (__hip_bfloat16*)d_out);
}
```

```cpp
#include <hip/hip_runtime.h>
#include <hip/hip_bf16.h>
#include <stdint.h>

typedef unsigned u32x4 __attribute__((ext_vector_type(4)));
typedef unsigned u32x2 __attribute__((ext_vector_type(2)));
typedef float f32x4 __attribute__((ext_vector_type(4)));
typedef __bf16 bf16x2 __attribute__((ext_vector_type(2)));
typedef __amdgpu_buffer_rsrc_t rsrc_t;
#define GAS __attribute__((address_space(1)))

constexpr int D = 640, H = 32, SK = 32768, DH = 192, W = 4096, F = 1536;
constexpr int NS = 8;
constexpr int AUX_NT = 2, AUX_SC1 = 16;
#define KV_AUX AUX_NT
constexpr int REC1 = 2560;
constexpr int WS_R1 = 4096, WS_R2 = WS_R1 + 256 * REC1, REC2 = 640, WS_END = WS_R2 + 256 * REC2;
constexpr unsigned SPIN_CAP = 1u << 20;

#define STAMP(i) do {} while (0)

__device__ __forceinline__ rsrc_t mk(const void* p, int bytes) { return __builtin_amdgcn_make_buffer_rsrc((void*)p, 0, bytes, 0x00020000); }
template <int AUX> __device__ __forceinline__ u32x4 ld16(rsrc_t r, int voff, int soff) { return __builtin_amdgcn_raw_buffer_load_b128(r, voff, soff, AUX); }
__device__ __forceinline__ float dot2(unsigned a, unsigned b, float c) {
  return __builtin_amdgcn_fdot2_f32_bf16(__builtin_bit_cast(bf16x2, a), __builtin_bit_cast(bf16x2, b), c, false);
}
__device__ __forceinline__ unsigned perm_lo(unsigned a, unsigned b) { return __builtin_amdgcn_perm(b, a, 0x05040100u); }
__device__ __forceinline__ unsigned perm_hi(unsigned a, unsigned b) { return __builtin_amdgcn_perm(b, a, 0x07060302u); }
__device__ __forceinline__ unsigned pack_bf16(float lo, float hi) { bf16x2 r = {(__bf16)lo, (__bf16)hi}; return __builtin_bit_cast(unsigned, r); }
__device__ __forceinline__ float bflo(unsigned u) { return __uint_as_float(u << 16); }
__device__ __forceinline__ float bfhi(unsigned u) { return __uint_as_float(u & 0xffff0000u); }
template <int CTRL> __device__ __forceinline__ float dpp(float x) {
  return __builtin_bit_cast(float, __builtin_amdgcn_mov_dpp(__builtin_bit_cast(int, x), CTRL, 0xf, 0xf, true));
}
constexpr int XOR1 = 0xB1, XOR2 = 0x4E, XOR7 = 0x141, XOR8 = 0x128;
__device__ __forceinline__ float tr32(float a, float b) { auto s = __builtin_amdgcn_permlane32_swap(__float_as_uint(a), __float_as_uint(b), false, false); return __uint_as_float(s[0]) + __uint_as_float(s[1]); }
__device__ __forceinline__ float tr16(float a, float b) { auto s = __builtin_amdgcn_permlane16_swap(__float_as_uint(a), __float_as_uint(b), false, false); return __uint_as_float(s[0]) + __uint_as_float(s[1]); }
__device__ __forceinline__ float max32(float a) { auto s = __builtin_amdgcn_permlane32_swap(__float_as_uint(a), __float_as_uint(a), false, false); return fmaxf(__uint_as_float(s[0]), __uint_as_float(s[1])); }
__device__ __forceinline__ float max16(float a) { auto s = __builtin_amdgcn_permlane16_swap(__float_as_uint(a), __float_as_uint(a), false, false); return fmaxf(__uint_as_float(s[0]), __uint_as_float(s[1])); }
__device__ __forceinline__ float sum_r8(float a) { a += dpp<XOR8>(a); a = tr16(a, a); return tr32(a, a); }
__device__ __forceinline__ float max_r8(float a) { a = fmaxf(a, dpp<XOR8>(a)); a = max16(a); return max32(a); }
#define SB() __builtin_amdgcn_sched_barrier(0)
#define LDS_BARRIER() do { asm volatile("s_waitcnt lgkmcnt(0)" ::: "memory"); __builtin_amdgcn_s_barrier(); asm volatile("" ::: "memory"); } while (0)

__device__ __forceinline__ void reduce24_store(float (&a)[24], float* dst, int lane) {
  float b[12], c[6];
#pragma unroll
  for (int i = 0; i < 12; ++i) b[i] = tr32(a[i], a[i + 12]);
#pragma unroll
  for (int i = 0; i < 6; ++i) c[i] = tr16(b[i], b[i + 6]);
  const bool hi = lane & 8;
  const int base = ((lane >> 5) & 1) * 12 + ((lane >> 4) & 1) * 6 + (hi ? 3 : 0);
#pragma unroll
  for (int i = 0; i < 3; ++i) {
    const float t1 = c[i] + dpp<XOR8>(c[i]), t2 = c[i + 3] + dpp<XOR8>(c[i + 3]);
    const int v = base + i;
    dst[(v >> 3) * 64 + (lane & 7) * 8 + (v & 7)] = hi ? t2 : t1;
  }
}

struct __attribute__((aligned(16))) Smem {
  float red[8][192];
  float att[8][200];
  float ffn[48][96];
  unsigned qb[8][96];
  float z[256];
  float y[8][80];
};

__global__ void __launch_bounds__(512, 2) fused_kernel(const unsigned short* __restrict__ x, const unsigned short* __restrict__ kc, const unsigned short* __restrict__ vc,
                                                      const unsigned short* __restrict__ wq, const unsigned short* __restrict__ wo, const unsigned short* __restrict__ wg,
                                                      const unsigned short* __restrict__ wu, const unsigned short* __restrict__ wd, const unsigned short* __restrict__ gw,
                                                      unsigned short* __restrict__ out, unsigned char* ws
) {
  __shared__ Smem sm;
  const int tid = threadIdx.x, lane = tid & 63, w = __builtin_amdgcn_readfirstlane(tid >> 6);
  const int b = blockIdx.x, h = (b & 7) * 4 + (b >> 6), s = (b >> 3) & 7;
  const int r8 = lane >> 3, c8 = lane & 7;
  STAMP(0);
  const size_t kvoff = ((size_t)h * SK + (SK - W) + s * 512 + w * 64) * DH;
  const rsrc_t rK = mk(kc + kvoff, 64 * DH * 2), rV = mk(vc + kvoff, 64 * DH * 2);
  const rsrc_t rQ = mk(wq + (size_t)(w * 80) * (H * DH) + h * DH, 80 * H * DH * 2 - h * DH * 2);
  const rsrc_t rW = mk(ws, WS_END);
  const int kvv = r8 * (DH * 2) + c8 * 16;
  const int qv = r8 * 10 * (H * DH * 2) + c8 * 16;

  u32x4 kv[24], wr[30];
#define LDK(g) _Pragma("unroll") for (int c = 0; c < 3; ++c) kv[(g) * 3 + c] = ld16<KV_AUX>(rK, kvv, (g) * 3072 + c * 128)
#define LDV(g) _Pragma("unroll") for (int c = 0; c < 3; ++c) kv[(g) * 3 + c] = ld16<KV_AUX>(rV, kvv, (g) * 3072 + c * 128)
#define LDQ(i) _Pragma("unroll") for (int c = 0; c < 3; ++c) wr[(i) * 3 + c] = ld16<0>(rQ, qv, (i) * (H * DH * 2) + c * 128)
  LDK(0); SB();
  const rsrc_t rX = mk(x, D * 2);
  const u32x4 xa = ld16<0>(rX, w * 160 + r8 * 20, 0);
  const unsigned xb = __builtin_amdgcn_raw_buffer_load_b32(rX, w * 160 + r8 * 20 + 16, 0, 0);
  const unsigned epoch = __builtin_amdgcn_raw_buffer_load_b32(rW, 0, 0, AUX_SC1);
  SB();
  LDQ(0); LDQ(1); LDQ(2); SB(); LDK(1); SB(); LDQ(3); LDQ(4); LDQ(5); SB(); LDK(2); SB(); LDQ(6); LDQ(7); LDQ(8); LDQ(9); SB(); LDK(3); SB();
  const int rg = lane / 12, mp = lane % 12;
  u32x4 fa, fb; unsigned xf;
  {
    const unsigned short* wm = mp < 6 ? wg : wu;
    const int row = 80 * s + 10 * w + 2 * (rg < 5 ? rg : 4);
    const GAS u32x4* p = (const GAS u32x4*)(wm + (size_t)row * F + 48 * h + (mp % 6) * 8);
    fa = p[0]; fb = p[F / 8];
    xf = __builtin_amdgcn_raw_buffer_load_b32(rX, row * 2, 0, 0);
  }
  const rsrc_t rG = mk(gw, D * 2);
  const u32x4 ga = ld16<0>(rG, lane * 20, 0), gxa = ld16<0>(rX, lane * 20, 0);
  const unsigned gb = __builtin_amdgcn_raw_buffer_load_b32(rG, lane * 20 + 16, 0, 0), gxb = __builtin_amdgcn_raw_buffer_load_b32(rX, lane * 20 + 16, 0, 0);
  SB();
  STAMP(1);
  float acc[24];
#pragma unroll
  for (int j = 0; j < 24; ++j) acc[j] = 0.f;
#define FMA_PAIR(A, B, P) _Pragma("unroll") for (int c = 0; c < 3; ++c) _Pragma("unroll") for (int j = 0; j < 4; ++j) { \
    acc[c * 8 + 2 * j] = dot2(perm_lo((A)[c][j], (B)[c][j]), P, acc[c * 8 + 2 * j]); \
    acc[c * 8 + 2 * j + 1] = dot2(perm_hi((A)[c][j], (B)[c][j]), P, acc[c * 8 + 2 * j + 1]); }
  FMA_PAIR(wr + 0, wr + 3, xa[0]); SB(); LDK(4); SB();
  FMA_PAIR(wr + 6, wr + 9, xa[1]); SB(); LDK(5); SB();
  FMA_PAIR(wr + 12, wr + 15, xa[2]); SB(); LDK(6); SB();
  FMA_PAIR(wr + 18, wr + 21, xa[3]); SB(); LDK(7); SB();
  FMA_PAIR(wr + 24, wr + 27, xb); SB();
  STAMP(2);
  reduce24_store(acc, sm.red[w], lane);
  {
    {
      f32x4 lo, hi;
#pragma unroll
      for (int j = 0; j < 2; ++j) {
        lo[2 * j] = dot2(perm_lo(fa[j], fb[j]), xf, 0.f); lo[2 * j + 1] = dot2(perm_hi(fa[j], fb[j]), xf, 0.f);
        hi[2 * j] = dot2(perm_lo(fa[j + 2], fb[j + 2]), xf, 0.f); hi[2 * j + 1] = dot2(perm_hi(fa[j + 2], fb[j + 2]), xf, 0.f);
      }
      f32x4* dst = (f32x4*)&sm.ffn[w * 6 + rg][mp * 8];
      dst[0] = lo; dst[1] = hi;
    }
  }
  float gate;
  {
    float t = dot2(gb, gxb, 0.f);
#pragma unroll
    for (int j = 0; j < 4; ++j) t = dot2(ga[j], gxa[j], t);
    t += dpp<XOR1>(t); t += dpp<XOR2>(t); t += dpp<XOR7>(t); t = sum_r8(t);
    gate = 1.f / (1.f + __expf(-t));
  }
  LDS_BARRIER();
  unsigned q[12];
  {
    if (lane < 48) {
      f32x4 t = *(const f32x4*)&sm.red[0][lane * 4];
#pragma unroll
      for (int i = 1; i < 8; ++i) t += *(const f32x4*)&sm.red[i][lane * 4];
      *(u32x2*)&sm.qb[w][lane * 2] = u32x2{pack_bf16(t[0], t[1]), pack_bf16(t[2], t[3])};
    }
    asm volatile("s_waitcnt lgkmcnt(0)" ::: "memory");
#pragma unroll
    for (int c = 0; c < 3; ++c) { const u32x4 t = *(const u32x4*)&sm.qb[w][c * 32 + c8 * 4]; q[c * 4] = t[0]; q[c * 4 + 1] = t[1]; q[c * 4 + 2] = t[2]; q[c * 4 + 3] = t[3]; }
  }
  const unsigned tag = epoch + 1u;
  if (w == 7 && lane < 48) {
    float g0 = 0.f, u0 = 0.f;
#pragma unroll 2
    for (int i = 0; i < 8; ++i)
#pragma unroll
      for (int k = 0; k < 5; ++k) { g0 += sm.ffn[i * 6 + k][lane]; u0 += sm.ffn[i * 6 + k][48 + lane]; }
    __builtin_amdgcn_raw_buffer_store_b128(u32x4{__float_as_uint(g0), tag, __float_as_uint(u0), tag}, rW, WS_R1 + b * REC1 + (96 + lane) * 16, 0, AUX_SC1);
  }
  STAMP(3);
  float sc[8];
  constexpr float C2 = 0.07216878364870322f * 1.4426950408889634f;
#pragma unroll
  for (int g = 0; g < 8; ++g) {
    float a0 = 0.f, a1 = 0.f;
#pragma unroll
    for (int c = 0; c < 3; ++c) { a0 = dot2(kv[g * 3 + c][0], q[c * 4], a0); a1 = dot2(kv[g * 3 + c][1], q[c * 4 + 1], a1); a0 = dot2(kv[g * 3 + c][2], q[c * 4 + 2], a0); a1 = dot2(kv[g * 3 + c][3], q[c * 4 + 3], a1); }
    SB(); LDV(g); SB();
    a0 += a1; a0 += dpp<XOR1>(a0); a0 += dpp<XOR2>(a0); a0 += dpp<XOR7>(a0);
    sc[g] = a0 * C2;
  }
  STAMP(4);
  u32x4 wz[8];
  {
    const int rq = lane >> 4, pc = lane & 15;
    const rsrc_t rO = mk(wo + (size_t)(h * DH + 24 * w) * D + 80 * s, 23 * D * 2 + 160), rD = mk(wd + (size_t)(48 * h + 6 * w) * D + 80 * s, 5 * D * 2 + 160);
    const int vo = pc < 10 ? rq * 6 * D * 2 + pc * 16 : 0x40000000, vd = pc < 10 ? rq * 2 * D * 2 + pc * 16 : 0x40000000;
#pragma unroll
    for (int i = 0; i < 6; ++i) wz[i] = ld16<0>(rO, vo + i * D * 2, 0);
#pragma unroll
    for (int i = 0; i < 2; ++i) wz[6 + i] = ld16<0>(rD, vd + i * D * 2, 0);
  }
  SB();
  float m = fmaxf(fmaxf(fmaxf(sc[0], sc[1]), fmaxf(sc[2], sc[3])), fmaxf(fmaxf(sc[4], sc[5]), fmaxf(sc[6], sc[7])));
  m = max_r8(m);
  float lsum = 0.f; unsigned pp[4];
#pragma unroll
  for (int g = 0; g < 4; ++g) {
    const float p0 = __builtin_amdgcn_exp2f(sc[2 * g] - m), p1 = __builtin_amdgcn_exp2f(sc[2 * g + 1] - m);
    lsum += p0 + p1; pp[g] = pack_bf16(p0, p1);
  }
  lsum = sum_r8(lsum);
#pragma unroll
  for (int j = 0; j < 24; ++j) acc[j] = 0.f;
  FMA_PAIR(kv + 0, kv + 3, pp[0]); SB();
  FMA_PAIR(kv + 6, kv + 9, pp[1]); SB();
  FMA_PAIR(kv + 12, kv + 15, pp[2]); SB();
  FMA_PAIR(kv + 18, kv + 21, pp[3]); SB();
  STAMP(5);
  reduce24_store(acc, sm.att[w], lane);
  if (lane == 0) { sm.att[w][192] = m; sm.att[w][193] = lsum; }
  LDS_BARRIER();
  if (tid < 97) {
    float mw[8], M = -INFINITY;
#pragma unroll
    for (int i = 0; i < 8; ++i) { mw[i] = sm.att[i][192]; M = fmaxf(M, mw[i]); }
    float o0 = 0.f, o1 = 0.f;
#pragma unroll
    for (int i = 0; i < 8; ++i) { const float e = __builtin_amdgcn_exp2f(mw[i] - M); const float2 t = *(const float2*)&sm.att[i][2 * tid]; o0 += e * t.x; o1 += e * t.y; }
    if (tid == 96) o0 = M;
    __builtin_amdgcn_raw_buffer_store_b128(u32x4{__float_as_uint(o0), tag, __float_as_uint(o1), tag}, rW, WS_R1 + b * REC1 + (tid == 96 ? 144 : tid) * 16, 0, AUX_SC1);
  }
  STAMP(6);
  {
    const int rec = WS_R1 + ((b & ~56) + r8 * 8) * REC1;
    const int p0 = 18 * w + c8, p2 = 18 * w + 16 + (c8 & 1);
    u32x4 a0, a1, a2, st;
    for (unsigned spins = 0; spins < SPIN_CAP; ++spins) {
      asm volatile("" ::: "memory");
      a0 = ld16<AUX_SC1>(rW, rec + p0 * 16, 0); a1 = ld16<AUX_SC1>(rW, rec + p0 * 16 + 128, 0); a2 = ld16<AUX_SC1>(rW, rec + p2 * 16, 0); st = ld16<AUX_SC1>(rW, rec + 144 * 16, 0);
      const bool ok = a0[1] == tag && a0[3] == tag && a1[1] == tag && a1[3] == tag && a2[1] == tag && a2[3] == tag && st[1] == tag && st[3] == tag;
      if (__all(ok)) break;
    }
    STAMP(7);
    const float ms = __uint_as_float(st[0]), M = max_r8(ms), e = __builtin_amdgcn_exp2f(ms - M), L = sum_r8(e * __uint_as_float(st[2]));
    const float so = e * gate / L;
    auto fin = [&](const u32x4& a, int p, bool wr_) {
      const float f = p < 96 ? so : 1.f;
      const float v0 = sum_r8(f * __uint_as_float(a[0])), v1 = sum_r8(f * __uint_as_float(a[2]));
      if (wr_) {
        if (p < 96) *(float2*)&sm.z[2 * p] = float2{v0, v1};
        else { const float u = 0.7978845608028654f * (v0 + 0.044715f * v0 * v0 * v0); const float th = 1.f - 2.f / (1.f + __expf(2.f * u));
               sm.z[96 + p] = (1.f - gate) * 0.5f * v0 * (1.f + th) * v1; }
      }
    };
    fin(a0, p0, r8 == 0); fin(a1, p0 + 8, r8 == 0); fin(a2, p2, lane < 2);
    if (tid < 16) sm.z[240 + tid] = 0.f;
  }
  LDS_BARRIER();
  {
    const int rq = lane >> 4;
    float zz[8], y[8];
#pragma unroll
    for (int i = 0; i < 8; i += 2) { const float2 t = *(const float2*)&sm.z[i < 6 ? 24 * w + rq * 6 + i : 192 + 6 * w + rq * 2]; zz[i] = t.x; zz[i + 1] = t.y; }
#pragma unroll
    for (int j = 0; j < 8; ++j) y[j] = 0.f;
#pragma unroll
    for (int i = 0; i < 8; ++i)
#pragma unroll
      for (int j = 0; j < 4; ++j) { y[2 * j] += zz[i] * bflo(wz[i][j]); y[2 * j + 1] += zz[i] * bfhi(wz[i][j]); }
    float y4[4];
#pragma unroll
    for (int j = 0; j < 4; ++j) y4[j] = tr32(y[j], y[j + 4]);
    const float ya = tr16(y4[0], y4[2]), yb = tr16(y4[1], y4[3]);
    if ((lane & 15) < 10) *(float2*)&sm.y[w][(lane & 15) * 8 + (lane >> 5) * 4 + ((lane >> 4) & 1) * 2] = float2{ya, yb};
  }
  LDS_BARRIER();
  if (w == 0) {
    if (lane < 40) {
      float o0 = 0.f, o1 = 0.f;
#pragma unroll
      for (int i = 0; i < 8; ++i) { const float2 t = *(const float2*)&sm.y[i][2 * lane]; o0 += t.x; o1 += t.y; }
      __builtin_amdgcn_raw_buffer_store_b128(u32x4{__float_as_uint(o0), tag, __float_as_uint(o1), tag}, rW, WS_R2 + (s * 32 + h) * REC2 + lane * 16, 0, AUX_SC1);
    }
    STAMP(8);
    if (h < 5) {
      const int rec = WS_R2 + (s * 32 + r8) * REC2 + h * 128 + c8 * 16;
      u32x4 a0, a1, a2, a3;
      for (unsigned spins = 0; spins < SPIN_CAP; ++spins) {
        asm volatile("" ::: "memory");
        a0 = ld16<AUX_SC1>(rW, rec, 0); a1 = ld16<AUX_SC1>(rW, rec, 8 * REC2); a2 = ld16<AUX_SC1>(rW, rec, 16 * REC2); a3 = ld16<AUX_SC1>(rW, rec, 24 * REC2);
        const bool ok = a0[1] == tag && a0[3] == tag && a1[1] == tag && a1[3] == tag && a2[1] == tag && a2[3] == tag && a3[1] == tag && a3[3] == tag;
        if (__all(ok)) break;
      }
      float v0 = (__uint_as_float(a0[0]) + __uint_as_float(a1[0])) + (__uint_as_float(a2[0]) + __uint_as_float(a3[0]));
      float v1 = (__uint_as_float(a0[2]) + __uint_as_float(a1[2])) + (__uint_as_float(a2[2]) + __uint_as_float(a3[2]));
      v0 = sum_r8(v0); v1 = sum_r8(v1);
      if (lane < 8) *(GAS unsigned*)(out + 80 * s + 16 * h + 2 * lane) = pack_bf16(v0, v1);
      if (b == 0 && lane == 0) __builtin_amdgcn_raw_buffer_store_b32(tag, rW, 0, 0, AUX_SC1);
      STAMP(9);
    }
  }
}

extern "C" void kernel_launch(void* const* d_in, const int* in_sizes, int n_in, void* d_out, int out_size, void* d_ws, size_t ws_size, hipStream_t stream) {
  fused_kernel<<<256, 512, 0, stream>>>((const unsigned short*)d_in[0], (const unsigned short*)d_in[1], (const unsigned short*)d_in[2], (const unsigned short*)d_in[3],
                                        (const unsigned short*)d_in[4], (const unsigned short*)d_in[6], (const unsigned short*)d_in[7], (const unsigned short*)d_in[8],
                                        (const unsigned short*)d_in[9], (unsigned short*)d_out, (unsigned char*)d_ws
  );
}
```

```cpp
#include <hip/hip_runtime.h>
#include <hip/hip_bf16.h>
#include <stdint.h>

typedef unsigned u32x4 __attribute__((ext_vector_type(4)));
typedef unsigned u32x2 __attribute__((ext_vector_type(2)));
typedef float f32x4 __attribute__((ext_vector_type(4)));
typedef __bf16 bf16x2 __attribute__((ext_vector_type(2)));
typedef __amdgpu_buffer_rsrc_t rsrc_t;
#define GAS __attribute__((address_space(1)))

constexpr int D = 640, H = 32, SK = 32768, DH = 192, W = 4096, F = 1536;
constexpr int NS = 8;
constexpr int AUX_NT = 2, AUX_SC1 = 16;
#define KV_AUX AUX_NT
constexpr int REC1 = 2560;
constexpr int WS_R1 = 4096, WS_R2 = WS_R1 + 256 * REC1, REC2 = 640, WS_END = WS_R2 + 256 * REC2;
constexpr unsigned SPIN_CAP = 1u << 20;

#define STAMP(i) do {} while (0)

__device__ __forceinline__ rsrc_t mk(const void* p, int bytes) { return __builtin_amdgcn_make_buffer_rsrc((void*)p, 0, bytes, 0x00020000); }
template <int AUX> __device__ __forceinline__ u32x4 ld16(rsrc_t r, int voff, int soff) { return __builtin_amdgcn_raw_buffer_load_b128(r, voff, soff, AUX); }
__device__ __forceinline__ float dot2(unsigned a, unsigned b, float c) {
  return __builtin_amdgcn_fdot2_f32_bf16(__builtin_bit_cast(bf16x2, a), __builtin_bit_cast(bf16x2, b), c, false);
}
__device__ __forceinline__ unsigned perm_lo(unsigned a, unsigned b) { return __builtin_amdgcn_perm(b, a, 0x05040100u); }
__device__ __forceinline__ unsigned perm_hi(unsigned a, unsigned b) { return __builtin_amdgcn_perm(b, a, 0x07060302u); }
__device__ __forceinline__ unsigned pack_bf16(float lo, float hi) { bf16x2 r = {(__bf16)lo, (__bf16)hi}; return __builtin_bit_cast(unsigned, r); }
__device__ __forceinline__ float bflo(unsigned u) { return __uint_as_float(u << 16); }
__device__ __forceinline__ float bfhi(unsigned u) { return __uint_as_float(u & 0xffff0000u); }
template <int CTRL> __device__ __forceinline__ float dpp(float x) {
  return __builtin_bit_cast(float, __builtin_amdgcn_mov_dpp(__builtin_bit_cast(int, x), CTRL, 0xf, 0xf, true));
}
constexpr int XOR1 = 0xB1, XOR2 = 0x4E, XOR7 = 0x141, XOR8 = 0x128;
__device__ __forceinline__ float tr32(float a, float b) { auto s = __builtin_amdgcn_permlane32_swap(__float_as_uint(a), __float_as_uint(b), false, false); return __uint_as_float(s[0]) + __uint_as_float(s[1]); }
__device__ __forceinline__ float tr16(float a, float b) { auto s = __builtin_amdgcn_permlane16_swap(__float_as_uint(a), __float_as_uint(b), false, false); return __uint_as_float(s[0]) + __uint_as_float(s[1]); }
__device__ __forceinline__ float max32(float a) { auto s = __builtin_amdgcn_permlane32_swap(__float_as_uint(a), __float_as_uint(a), false, false); return fmaxf(__uint_as_float(s[0]), __uint_as_float(s[1])); }
__device__ __forceinline__ float max16(float a) { auto s = __builtin_amdgcn_permlane16_swap(__float_as_uint(a), __float_as_uint(a), false, false); return fmaxf(__uint_as_float(s[0]), __uint_as_float(s[1])); }
__device__ __forceinline__ float sum_r8(float a) { a += dpp<XOR8>(a); a = tr16(a, a); return tr32(a, a); }
__device__ __forceinline__ float max_r8(float a) { a = fmaxf(a, dpp<XOR8>(a)); a = max16(a); return max32(a); }
#define SB() __builtin_amdgcn_sched_barrier(0)
#define LDS_BARRIER() do { asm volatile("s_waitcnt lgkmcnt(0)" ::: "memory"); __builtin_amdgcn_s_barrier(); asm volatile("" ::: "memory"); } while (0)

__device__ __forceinline__ void reduce24_store(float (&a)[24], float* dst, int lane) {
  float b[12], c[6];
#pragma unroll
  for (int i = 0; i < 12; ++i) b[i] = tr32(a[i], a[i + 12]);
#pragma unroll
  for (int i = 0; i < 6; ++i) c[i] = tr16(b[i], b[i + 6]);
  const bool hi = lane & 8;
  const int base = ((lane >> 5) & 1) * 12 + ((lane >> 4) & 1) * 6 + (hi ? 3 : 0);
#pragma unroll
  for (int i = 0; i < 3; ++i) {
    const float t1 = c[i] + dpp<XOR8>(c[i]), t2 = c[i + 3] + dpp<XOR8>(c[i + 3]);
    const int v = base + i;
    dst[(v >> 3) * 64 + (lane & 7) * 8 + (v & 7)] = hi ? t2 : t1;
  }
}

struct __attribute__((aligned(16))) Smem {
  float red[8][192];
  float att[8][200];
  float ffn[48][96];
  unsigned qb[8][96];
  float z[256];
  float y[8][80];
};

__global__ void __launch_bounds__(512, 2) fused_kernel(const unsigned short* __restrict__ x, const unsigned short* __restrict__ kc, const unsigned short* __restrict__ vc,
                                                      const unsigned short* __restrict__ wq, const unsigned short* __restrict__ wo, const unsigned short* __restrict__ wg,
                                                      const unsigned short* __restrict__ wu, const unsigned short* __restrict__ wd, const unsigned short* __restrict__ gw,
                                                      unsigned short* __restrict__ out, unsigned char* ws
) {
  __shared__ Smem sm;
  const int tid = threadIdx.x, lane = tid & 63, w = __builtin_amdgcn_readfirstlane(tid >> 6);
  const int b = blockIdx.x, h = (b & 7) * 4 + (b >> 6), s = (b >> 3) & 7;
  const int r8 = lane >> 3, c8 = lane & 7;
  STAMP(0);
  const size_t kvoff = ((size_t)h * SK + (SK - W) + s * 512 + w * 64) * DH;
  const rsrc_t rK = mk(kc + kvoff, 64 * DH * 2), rV = mk(vc + kvoff, 64 * DH * 2);
  const rsrc_t rQ = mk(wq + (size_t)(w * 80) * (H * DH) + h * DH, 80 * H * DH * 2 - h * DH * 2);
  const rsrc_t rW = mk(ws, WS_END);
  const int kvv = r8 * (DH * 2) + c8 * 16;
  const int qv = r8 * 10 * (H * DH * 2) + c8 * 16;

  u32x4 kv[24], wr[30];
#define LDK(g) _Pragma("unroll") for (int c = 0; c < 3; ++c) kv[(g) * 3 + c] = ld16<KV_AUX>(rK, kvv, (g) * 3072 + c * 128)
#define LDV(g) _Pragma("unroll") for (int c = 0; c < 3; ++c) kv[(g) * 3 + c] = ld16<KV_AUX>(rV, kvv, (g) * 3072 + c * 128)
#define LDQ(i) _Pragma("unroll") for (int c = 0; c < 3; ++c) wr[(i) * 3 + c] = ld16<AUX_NT>(rQ, qv, (i) * (H * DH * 2) + c * 128)
  LDK(0); SB();
  const rsrc_t rX = mk(x, D * 2);
  const u32x4 xa = ld16<0>(rX, w * 160 + r8 * 20, 0);
  const unsigned xb = __builtin_amdgcn_raw_buffer_load_b32(rX, w * 160 + r8 * 20 + 16, 0, 0);
  const unsigned epoch = __builtin_amdgcn_raw_buffer_load_b32(rW, 0, 0, AUX_SC1);
  SB();
  LDQ(0); LDQ(1); LDQ(2); SB(); LDK(1); SB(); LDQ(3); LDQ(4); LDQ(5); SB(); LDK(2); SB(); LDQ(6); LDQ(7); LDQ(8); LDQ(9); SB(); LDK(3); SB();
  const int rg = lane / 12, mp = lane % 12;
  u32x4 fa, fb; unsigned xf;
  {
    const unsigned short* wm = mp < 6 ? wg : wu;
    const int row = 80 * s + 10 * w + 2 * (rg < 5 ? rg : 4);
    const GAS u32x4* p = (const GAS u32x4*)(wm + (size_t)row * F + 48 * h + (mp % 6) * 8);
    fa = __builtin_nontemporal_load(p); fb = __builtin_nontemporal_load(p + F / 8);
    xf = __builtin_amdgcn_raw_buffer_load_b32(rX, row * 2, 0, 0);
  }
  const rsrc_t rG = mk(gw, D * 2);
  const u32x4 ga = ld16<0>(rG, lane * 20, 0), gxa = ld16<0>(rX, lane * 20, 0);
  const unsigned gb = __builtin_amdgcn_raw_buffer_load_b32(rG, lane * 20 + 16, 0, 0), gxb = __builtin_amdgcn_raw_buffer_load_b32(rX, lane * 20 + 16, 0, 0);
  SB();
  STAMP(1);
  float acc[24];
#pragma unroll
  for (int j = 0; j < 24; ++j) acc[j] = 0.f;
#define FMA_PAIR(A, B, P) _Pragma("unroll") for (int c = 0; c < 3; ++c) _Pragma("unroll") for (int j = 0; j < 4; ++j) { \
    acc[c * 8 + 2 * j] = dot2(perm_lo((A)[c][j], (B)[c][j]), P, acc[c * 8 + 2 * j]); \
    acc[c * 8 + 2 * j + 1] = dot2(perm_hi((A)[c][j], (B)[c][j]), P, acc[c * 8 + 2 * j + 1]); }
  FMA_PAIR(wr + 0, wr + 3, xa[0]); SB(); LDK(4); SB();
  FMA_PAIR(wr + 6, wr + 9, xa[1]); SB(); LDK(5); SB();
  FMA_PAIR(wr + 12, wr + 15, xa[2]); SB(); LDK(6); SB();
  FMA_PAIR(wr + 18, wr + 21, xa[3]); SB(); LDK(7); SB();
  FMA_PAIR(wr + 24, wr + 27, xb); SB();
  STAMP(2);
  reduce24_store(acc, sm.red[w], lane);
  {
    {
      f32x4 lo, hi;
#pragma unroll
      for (int j = 0; j < 2; ++j) {
        lo[2 * j] = dot2(perm_lo(fa[j], fb[j]), xf, 0.f); lo[2 * j + 1] = dot2(perm_hi(fa[j], fb[j]), xf, 0.f);
        hi[2 * j] = dot2(perm_lo(fa[j + 2], fb[j + 2]), xf, 0.f); hi[2 * j + 1] = dot2(perm_hi(fa[j + 2], fb[j + 2]), xf, 0.f);
      }
      f32x4* dst = (f32x4*)&sm.ffn[w * 6 + rg][mp * 8];
      dst[0] = lo; dst[1] = hi;
    }
  }
  float gate;
  {
    float t = dot2(gb, gxb, 0.f);
#pragma unroll
    for (int j = 0; j < 4; ++j) t = dot2(ga[j], gxa[j], t);
    t += dpp<XOR1>(t); t += dpp<XOR2>(t); t += dpp<XOR7>(t); t = sum_r8(t);
    gate = 1.f / (1.f + __expf(-t));
  }
  LDS_BARRIER();
  unsigned q[12];
  {
    if (lane < 48) {
      f32x4 t = *(const f32x4*)&sm.red[0][lane * 4];
#pragma unroll
      for (int i = 1; i < 8; ++i) t += *(const f32x4*)&sm.red[i][lane * 4];
      *(u32x2*)&sm.qb[w][lane * 2] = u32x2{pack_bf16(t[0], t[1]), pack_bf16(t[2], t[3])};
    }
    asm volatile("s_waitcnt lgkmcnt(0)" ::: "memory");
#pragma unroll
    for (int c = 0; c < 3; ++c) { const u32x4 t = *(const u32x4*)&sm.qb[w][c * 32 + c8 * 4]; q[c * 4] = t[0]; q[c * 4 + 1] = t[1]; q[c * 4 + 2] = t[2]; q[c * 4 + 3] = t[3]; }
  }
  const unsigned tag = epoch + 1u;
  if (w == 7 && lane < 48) {
    float g0 = 0.f, u0 = 0.f;
#pragma unroll 2
    for (int i = 0; i < 8; ++i)
#pragma unroll
      for (int k = 0; k < 5; ++k) { g0 += sm.ffn[i * 6 + k][lane]; u0 += sm.ffn[i * 6 + k][48 + lane]; }
    __builtin_amdgcn_raw_buffer_store_b128(u32x4{__float_as_uint(g0), tag, __float_as_uint(u0), tag}, rW, WS_R1 + b * REC1 + (96 + lane) * 16, 0, AUX_SC1);
  }
  STAMP(3);
  float sc[8];
  constexpr float C2 = 0.07216878364870322f * 1.4426950408889634f;
#pragma unroll
  for (int g = 0; g < 8; ++g) {
    float a0 = 0.f, a1 = 0.f;
#pragma unroll
    for (int c = 0; c < 3; ++c) { a0 = dot2(kv[g * 3 + c][0], q[c * 4], a0); a1 = dot2(kv[g * 3 + c][1], q[c * 4 + 1], a1); a0 = dot2(kv[g * 3 + c][2], q[c * 4 + 2], a0); a1 = dot2(kv[g * 3 + c][3], q[c * 4 + 3], a1); }
    SB(); LDV(g); SB();
    a0 += a1; a0 += dpp<XOR1>(a0); a0 += dpp<XOR2>(a0); a0 += dpp<XOR7>(a0);
    sc[g] = a0 * C2;
  }
  STAMP(4);
  u32x4 wz[8];
  {
    const int rq = lane >> 4, pc = lane & 15;
    const rsrc_t rO = mk(wo + (size_t)(h * DH + 24 * w) * D + 80 * s, 23 * D * 2 + 160), rD = mk(wd + (size_t)(48 * h + 6 * w) * D + 80 * s, 5 * D * 2 + 160);
    const int vo = pc < 10 ? rq * 6 * D * 2 + pc * 16 : 0x40000000, vd = pc < 10 ? rq * 2 * D * 2 + pc * 16 : 0x40000000;
#pragma unroll
    for (int i = 0; i < 6; ++i) wz[i] = ld16<AUX_NT>(rO, vo + i * D * 2, 0);
#pragma unroll
    for (int i = 0; i < 2; ++i) wz[6 + i] = ld16<AUX_NT>(rD, vd + i * D * 2, 0);
  }
  SB();
  float m = fmaxf(fmaxf(fmaxf(sc[0], sc[1]), fmaxf(sc[2], sc[3])), fmaxf(fmaxf(sc[4], sc[5]), fmaxf(sc[6], sc[7])));
  m = max_r8(m);
  float lsum = 0.f; unsigned pp[4];
#pragma unroll
  for (int g = 0; g < 4; ++g) {
    const float p0 = __builtin_amdgcn_exp2f(sc[2 * g] - m), p1 = __builtin_amdgcn_exp2f(sc[2 * g + 1] - m);
    lsum += p0 + p1; pp[g] = pack_bf16(p0, p1);
  }
  lsum = sum_r8(lsum);
#pragma unroll
  for (int j = 0; j < 24; ++j) acc[j] = 0.f;
  FMA_PAIR(kv + 0, kv + 3, pp[0]); SB();
  FMA_PAIR(kv + 6, kv + 9, pp[1]); SB();
  FMA_PAIR(kv + 12, kv + 15, pp[2]); SB();
  FMA_PAIR(kv + 18, kv + 21, pp[3]); SB();
  STAMP(5);
  reduce24_store(acc, sm.att[w], lane);
  if (lane == 0) { sm.att[w][192] = m; sm.att[w][193] = lsum; }
  LDS_BARRIER();
  if (tid < 97) {
    float mw[8], M = -INFINITY;
#pragma unroll
    for (int i = 0; i < 8; ++i) { mw[i] = sm.att[i][192]; M = fmaxf(M, mw[i]); }
    float o0 = 0.f, o1 = 0.f;
#pragma unroll
    for (int i = 0; i < 8; ++i) { const float e = __builtin_amdgcn_exp2f(mw[i] - M); const float2 t = *(const float2*)&sm.att[i][2 * tid]; o0 += e * t.x; o1 += e * t.y; }
    if (tid == 96) o0 = M;
    __builtin_amdgcn_raw_buffer_store_b128(u32x4{__float_as_uint(o0), tag, __float_as_uint(o1), tag}, rW, WS_R1 + b * REC1 + (tid == 96 ? 144 : tid) * 16, 0, AUX_SC1);
  }
  STAMP(6);
  {
    const int rec = WS_R1 + ((b & ~56) + r8 * 8) * REC1;
    const int p0 = 18 * w + c8, p2 = 18 * w + 16 + (c8 & 1);
    u32x4 a0, a1, a2, st;
    for (unsigned spins = 0; spins < SPIN_CAP; ++spins) {
      asm volatile("" ::: "memory");
      a0 = ld16<AUX_SC1>(rW, rec + p0 * 16, 0); a1 = ld16<AUX_SC1>(rW, rec + p0 * 16 + 128, 0); a2 = ld16<AUX_SC1>(rW, rec + p2 * 16, 0); st = ld16<AUX_SC1>(rW, rec + 144 * 16, 0);
      const bool ok = a0[1] == tag && a0[3] == tag && a1[1] == tag && a1[3] == tag && a2[1] == tag && a2[3] == tag && st[1] == tag && st[3] == tag;
      if (__all(ok)) break;
    }
    STAMP(7);
    const float ms = __uint_as_float(st[0]), M = max_r8(ms), e = __builtin_amdgcn_exp2f(ms - M), L = sum_r8(e * __uint_as_float(st[2]));
    const float so = e * gate / L;
    auto fin = [&](const u32x4& a, int p, bool wr_) {
      const float f = p < 96 ? so : 1.f;
      const float v0 = sum_r8(f * __uint_as_float(a[0])), v1 = sum_r8(f * __uint_as_float(a[2]));
      if (wr_) {
        if (p < 96) *(float2*)&sm.z[2 * p] = float2{v0, v1};
        else { const float u = 0.7978845608028654f * (v0 + 0.044715f * v0 * v0 * v0); const float th = 1.f - 2.f / (1.f + __expf(2.f * u));
               sm.z[96 + p] = (1.f - gate) * 0.5f * v0 * (1.f + th) * v1; }
      }
    };
    fin(a0, p0, r8 == 0); fin(a1, p0 + 8, r8 == 0); fin(a2, p2, lane < 2);
    if (tid < 16) sm.z[240 + tid] = 0.f;
  }
  LDS_BARRIER();
  {
    const int rq = lane >> 4;
    float zz[8], y[8];
#pragma unroll
    for (int i = 0; i < 8; i += 2) { const float2 t = *(const float2*)&sm.z[i < 6 ? 24 * w + rq * 6 + i : 192 + 6 * w + rq * 2]; zz[i] = t.x; zz[i + 1] = t.y; }
#pragma unroll
    for (int j = 0; j < 8; ++j) y[j] = 0.f;
#pragma unroll
    for (int i = 0; i < 8; ++i)
#pragma unroll
      for (int j = 0; j < 4; ++j) { y[2 * j] += zz[i] * bflo(wz[i][j]); y[2 * j + 1] += zz[i] * bfhi(wz[i][j]); }
    float y4[4];
#pragma unroll
    for (int j = 0; j < 4; ++j) y4[j] = tr32(y[j], y[j + 4]);
    const float ya = tr16(y4[0], y4[2]), yb = tr16(y4[1], y4[3]);
    if ((lane & 15) < 10) *(float2*)&sm.y[w][(lane & 15) * 8 + (lane >> 5) * 4 + ((lane >> 4) & 1) * 2] = float2{ya, yb};
  }
  LDS_BARRIER();
  if (w == 0) {
    if (lane < 40) {
      float o0 = 0.f, o1 = 0.f;
#pragma unroll
      for (int i = 0; i < 8; ++i) { const float2 t = *(const float2*)&sm.y[i][2 * lane]; o0 += t.x; o1 += t.y; }
      __builtin_amdgcn_raw_buffer_store_b128(u32x4{__float_as_uint(o0), tag, __float_as_uint(o1), tag}, rW, WS_R2 + (s * 32 + h) * REC2 + lane * 16, 0, AUX_SC1);
    }
    STAMP(8);
    if (h < 5) {
      const int rec = WS_R2 + (s * 32 + r8) * REC2 + h * 128 + c8 * 16;
      u32x4 a0, a1, a2, a3;
      for (unsigned spins = 0; spins < SPIN_CAP; ++spins) {
        asm volatile("" ::: "memory");
        a0 = ld16<AUX_SC1>(rW, rec, 0); a1 = ld16<AUX_SC1>(rW, rec, 8 * REC2); a2 = ld16<AUX_SC1>(rW, rec, 16 * REC2); a3 = ld16<AUX_SC1>(rW, rec, 24 * REC2);
        const bool ok = a0[1] == tag && a0[3] == tag && a1[1] == tag && a1[3] == tag && a2[1] == tag && a2[3] == tag && a3[1] == tag && a3[3] == tag;
        if (__all(ok)) break;
      }
      float v0 = (__uint_as_float(a0[0]) + __uint_as_float(a1[0])) + (__uint_as_float(a2[0]) + __uint_as_float(a3[0]));
      float v1 = (__uint_as_float(a0[2]) + __uint_as_float(a1[2])) + (__uint_as_float(a2[2]) + __uint_as_float(a3[2]));
      v0 = sum_r8(v0); v1 = sum_r8(v1);
      if (lane < 8) *(GAS unsigned*)(out + 80 * s + 16 * h + 2 * lane) = pack_bf16(v0, v1);
      if (b == 0 && lane == 0) __builtin_amdgcn_raw_buffer_store_b32(tag, rW, 0, 0, AUX_SC1);
      STAMP(9);
    }
  }
}

extern "C" void kernel_launch(void* const* d_in, const int* in_sizes, int n_in, void* d_out, int out_size, void* d_ws, size_t ws_size, hipStream_t stream) {
  fused_kernel<<<256, 512, 0, stream>>>((const unsigned short*)d_in[0], (const unsigned short*)d_in[1], (const unsigned short*)d_in[2], (const unsigned short*)d_in[3],
                                        (const unsigned short*)d_in[4], (const unsigned short*)d_in[6], (const unsigned short*)d_in[7], (const unsigned short*)d_in[8],
                                        (const unsigned short*)d_in[9], (unsigned short*)d_out, (unsigned char*)d_ws
  );
}
```

```cpp
#include <hip/hip_runtime.h>
#include <hip/hip_bf16.h>
#include <stdint.h>
#include <stdio.h>

typedef unsigned u32x4 __attribute__((ext_vector_type(4)));
typedef unsigned u32x2 __attribute__((ext_vector_type(2)));
typedef float f32x4 __attribute__((ext_vector_type(4)));
typedef __bf16 bf16x2 __attribute__((ext_vector_type(2)));
typedef __amdgpu_buffer_rsrc_t rsrc_t;
#define GAS __attribute__((address_space(1)))

constexpr int D = 640, H = 32, SK = 32768, DH = 192, W = 4096, F = 1536;
constexpr int NS = 8;
constexpr int AUX_NT = 2, AUX_SC1 = 16;
#define KV_AUX AUX_NT
constexpr int REC1 = 2560;
constexpr int WS_R1 = 4096, WS_R2 = WS_R1 + 256 * REC1, REC2 = 640, WS_END = WS_R2 + 256 * REC2;
constexpr unsigned SPIN_CAP = 1u << 20;

#define STAMP(i) do {} while (0)

__device__ __forceinline__ rsrc_t mk(const void* p, int bytes) { return __builtin_amdgcn_make_buffer_rsrc((void*)p, 0, bytes, 0x00020000); }
template <int AUX> __device__ __forceinline__ u32x4 ld16(rsrc_t r, int voff, int soff) { return __builtin_amdgcn_raw_buffer_load_b128(r, voff, soff, AUX); }
__device__ __forceinline__ float dot2(unsigned a, unsigned b, float c) {
  return __builtin_amdgcn_fdot2_f32_bf16(__builtin_bit_cast(bf16x2, a), __builtin_bit_cast(bf16x2, b), c, false);
}
__device__ __forceinline__ unsigned perm_lo(unsigned a, unsigned b) { return __builtin_amdgcn_perm(b, a, 0x05040100u); }
__device__ __forceinline__ unsigned perm_hi(unsigned a, unsigned b) { return __builtin_amdgcn_perm(b, a, 0x07060302u); }
__device__ __forceinline__ unsigned pack_bf16(float lo, float hi) { bf16x2 r = {(__bf16)lo, (__bf16)hi}; return __builtin_bit_cast(unsigned, r); }
__device__ __forceinline__ float bflo(unsigned u) { return __uint_as_float(u << 16); }
__device__ __forceinline__ float bfhi(unsigned u) { return __uint_as_float(u & 0xffff0000u); }
template <int CTRL> __device__ __forceinline__ float dpp(float x) {
  return __builtin_bit_cast(float, __builtin_amdgcn_mov_dpp(__builtin_bit_cast(int, x), CTRL, 0xf, 0xf, true));
}
constexpr int XOR1 = 0xB1, XOR2 = 0x4E, XOR7 = 0x141, XOR8 = 0x128;
__device__ __forceinline__ float tr32(float a, float b) { auto s = __builtin_amdgcn_permlane32_swap(__float_as_uint(a), __float_as_uint(b), false, false); return __uint_as_float(s[0]) + __uint_as_float(s[1]); }
__device__ __forceinline__ float tr16(float a, float b) { auto s = __builtin_amdgcn_permlane16_swap(__float_as_uint(a), __float_as_uint(b), false, false); return __uint_as_float(s[0]) + __uint_as_float(s[1]); }
__device__ __forceinline__ float max32(float a) { auto s = __builtin_amdgcn_permlane32_swap(__float_as_uint(a), __float_as_uint(a), false, false); return fmaxf(__uint_as_float(s[0]), __uint_as_float(s[1])); }
__device__ __forceinline__ float max16(float a) { auto s = __builtin_amdgcn_permlane16_swap(__float_as_uint(a), __float_as_uint(a), false, false); return fmaxf(__uint_as_float(s[0]), __uint_as_float(s[1])); }
__device__ __forceinline__ float sum_r8(float a) { a += dpp<XOR8>(a); a = tr16(a, a); return tr32(a, a); }
__device__ __forceinline__ float max_r8(float a) { a = fmaxf(a, dpp<XOR8>(a)); a = max16(a); return max32(a); }
#define SB() __builtin_amdgcn_sched_barrier(0)
#define LDS_BARRIER() do { asm volatile("s_waitcnt lgkmcnt(0)" ::: "memory"); __builtin_amdgcn_s_barrier(); asm volatile("" ::: "memory"); } while (0)

__device__ __forceinline__ void reduce24_store(float (&a)[24], float* dst, int lane) {
  float b[12], c[6];
#pragma unroll
  for (int i = 0; i < 12; ++i) b[i] = tr32(a[i], a[i + 12]);
#pragma unroll
  for (int i = 0; i < 6; ++i) c[i] = tr16(b[i], b[i + 6]);
  const bool hi = lane & 8;
  const int base = ((lane >> 5) & 1) * 12 + ((lane >> 4) & 1) * 6 + (hi ? 3 : 0);
#pragma unroll
  for (int i = 0; i < 3; ++i) {
    const float t1 = c[i] + dpp<XOR8>(c[i]), t2 = c[i + 3] + dpp<XOR8>(c[i + 3]);
    const int v = base + i;
    dst[(v >> 3) * 64 + (lane & 7) * 8 + (v & 7)] = hi ? t2 : t1;
  }
}

struct __attribute__((aligned(16))) Smem {
  float red[8][192];
  float att[8][200];
  float ffn[48][96];
  unsigned qb[8][96];
  float z[256];
  float y[8][80];
};

__global__ void __launch_bounds__(512, 2) fused_kernel(const unsigned short* __restrict__ x, const unsigned short* __restrict__ kc, const unsigned short* __restrict__ vc,
                                                      const unsigned short* __restrict__ wq, const unsigned short* __restrict__ wo, const unsigned short* __restrict__ wg,
                                                      const unsigned short* __restrict__ wu, const unsigned short* __restrict__ wd, const unsigned short* __restrict__ gw,
                                                      unsigned short* __restrict__ out, unsigned char* ws
) {
  __shared__ Smem sm;
  const int tid = threadIdx.x, lane = tid & 63, w = __builtin_amdgcn_readfirstlane(tid >> 6);
  const int b = blockIdx.x, h = (b & 7) * 4 + (b >> 6), s = (b >> 3) & 7;
  const int r8 = lane >> 3, c8 = lane & 7;
  STAMP(0);
  const size_t kvoff = ((size_t)h * SK + (SK - W) + s * 512 + w * 64) * DH;
  const rsrc_t rK = mk(kc + kvoff, 64 * DH * 2), rV = mk(vc + kvoff, 64 * DH * 2);
  const rsrc_t rQ = mk(wq + (size_t)(w * 80) * (H * DH) + h * DH, 80 * H * DH * 2 - h * DH * 2);
  const rsrc_t rW = mk(ws, WS_END);
  const int kvv = r8 * (DH * 2) + c8 * 16;
  const int qv = r8 * 10 * (H * DH * 2) + c8 * 16;

  u32x4 kv[24], wr[30];
#define LDK(g) _Pragma("unroll") for (int c = 0; c < 3; ++c) kv[(g) * 3 + c] = ld16<KV_AUX>(rK, kvv, (g) * 3072 + c * 128)
#define LDV(g) _Pragma("unroll") for (int c = 0; c < 3; ++c) kv[(g) * 3 + c] = ld16<KV_AUX>(rV, kvv, (g) * 3072 + c * 128)
#define LDQ(i) _Pragma("unroll") for (int c = 0; c < 3; ++c) wr[(i) * 3 + c] = ld16<AUX_NT>(rQ, qv, (i) * (H * DH * 2) + c * 128)
  LDK(0); SB();
  const rsrc_t rX = mk(x, D * 2);
  const u32x4 xa = ld16<0>(rX, w * 160 + r8 * 20, 0);
  const unsigned xb = __builtin_amdgcn_raw_buffer_load_b32(rX, w * 160 + r8 * 20 + 16, 0, 0);
  const unsigned epoch = __builtin_amdgcn_raw_buffer_load_b32(rW, 0, 0, AUX_SC1);
  SB();
  LDQ(0); LDQ(1); LDQ(2); SB(); LDK(1); SB(); LDQ(3); LDQ(4); LDQ(5); SB(); LDK(2); SB(); LDQ(6); LDQ(7); LDQ(8); LDQ(9); SB(); LDK(3); SB();
  const int rg = lane / 12, mp = lane % 12;
  u32x4 fa, fb; unsigned xf;
  {
    const unsigned short* wm = mp < 6 ? wg : wu;
    const int row = 80 * s + 10 * w + 2 * (rg < 5 ? rg : 4);
    const GAS u32x4* p = (const GAS u32x4*)(wm + (size_t)row * F + 48 * h + (mp % 6) * 8);
    fa = __builtin_nontemporal_load(p); fb = __builtin_nontemporal_load(p + F / 8);
    xf = __builtin_amdgcn_raw_buffer_load_b32(rX, row * 2, 0, 0);
  }
  const rsrc_t rG = mk(gw, D * 2);
  const u32x4 ga = ld16<0>(rG, lane * 20, 0), gxa = ld16<0>(rX, lane * 20, 0);
  const unsigned gb = __builtin_amdgcn_raw_buffer_load_b32(rG, lane * 20 + 16, 0, 0), gxb = __builtin_amdgcn_raw_buffer_load_b32(rX, lane * 20 + 16, 0, 0);
  SB();
  STAMP(1);
  float acc[24];
#pragma unroll
  for (int j = 0; j < 24; ++j) acc[j] = 0.f;
#define FMA_PAIR(A, B, P) _Pragma("unroll") for (int c = 0; c < 3; ++c) _Pragma("unroll") for (int j = 0; j < 4; ++j) { \
    acc[c * 8 + 2 * j] = dot2(perm_lo((A)[c][j], (B)[c][j]), P, acc[c * 8 + 2 * j]); \
    acc[c * 8 + 2 * j + 1] = dot2(perm_hi((A)[c][j], (B)[c][j]), P, acc[c * 8 + 2 * j + 1]); }
  FMA_PAIR(wr + 0, wr + 3, xa[0]); SB(); LDK(4); SB();
  FMA_PAIR(wr + 6, wr + 9, xa[1]); SB(); LDK(5); SB();
  FMA_PAIR(wr + 12, wr + 15, xa[2]); SB(); LDK(6); SB();
  FMA_PAIR(wr + 18, wr + 21, xa[3]); SB(); LDK(7); SB();
  FMA_PAIR(wr + 24, wr + 27, xb); SB();
  STAMP(2);
  reduce24_store(acc, sm.red[w], lane);
  {
    {
      f32x4 lo, hi;
#pragma unroll
      for (int j = 0; j < 2; ++j) {
        lo[2 * j] = dot2(perm_lo(fa[j], fb[j]), xf, 0.f); lo[2 * j + 1] = dot2(perm_hi(fa[j], fb[j]), xf, 0.f);
        hi[2 * j] = dot2(perm_lo(fa[j + 2], fb[j + 2]), xf, 0.f); hi[2 * j + 1] = dot2(perm_hi(fa[j + 2], fb[j + 2]), xf, 0.f);
      }
      f32x4* dst = (f32x4*)&sm.ffn[w * 6 + rg][mp * 8];
      dst[0] = lo; dst[1] = hi;
    }
  }
  float gate;
  {
    float t = dot2(gb, gxb, 0.f);
#pragma unroll
    for (int j = 0; j < 4; ++j) t = dot2(ga[j], gxa[j], t);
    t += dpp<XOR1>(t); t += dpp<XOR2>(t); t += dpp<XOR7>(t); t = sum_r8(t);
    gate = 1.f / (1.f + __expf(-t));
  }
  LDS_BARRIER();
  unsigned q[12];
  {
    if (lane < 48) {
      f32x4 t = *(const f32x4*)&sm.red[0][lane * 4];
#pragma unroll
      for (int i = 1; i < 8; ++i) t += *(const f32x4*)&sm.red[i][lane * 4];
      *(u32x2*)&sm.qb[w][lane * 2] = u32x2{pack_bf16(t[0], t[1]), pack_bf16(t[2], t[3])};
    }
    asm volatile("s_waitcnt lgkmcnt(0)" ::: "memory");
#pragma unroll
    for (int c = 0; c < 3; ++c) { const u32x4 t = *(const u32x4*)&sm.qb[w][c * 32 + c8 * 4]; q[c * 4] = t[0]; q[c * 4 + 1] = t[1]; q[c * 4 + 2] = t[2]; q[c * 4 + 3] = t[3]; }
  }
  const unsigned tag = epoch + 1u;
  if (w == 7 && lane < 48) {
    float g0 = 0.f, u0 = 0.f;
#pragma unroll 2
    for (int i = 0; i < 8; ++i)
#pragma unroll
      for (int k = 0; k < 5; ++k) { g0 += sm.ffn[i * 6 + k][lane]; u0 += sm.ffn[i * 6 + k][48 + lane]; }
    __builtin_amdgcn_raw_buffer_store_b128(u32x4{__float_as_uint(g0), tag, __float_as_uint(u0), tag}, rW, WS_R1 + b * REC1 + (96 + lane) * 16, 0, AUX_SC1);
  }
  STAMP(3);
  float sc[8];
  constexpr float C2 = 0.07216878364870322f * 1.4426950408889634f;
#pragma unroll
  for (int g = 0; g < 8; ++g) {
    float a0 = 0.f, a1 = 0.f;
#pragma unroll
    for (int c = 0; c < 3; ++c) { a0 = dot2(kv[g * 3 + c][0], q[c * 4], a0); a1 = dot2(kv[g * 3 + c][1], q[c * 4 + 1], a1); a0 = dot2(kv[g * 3 + c][2], q[c * 4 + 2], a0); a1 = dot2(kv[g * 3 + c][3], q[c * 4 + 3], a1); }
    SB(); LDV(g); SB();
    a0 += a1; a0 += dpp<XOR1>(a0); a0 += dpp<XOR2>(a0); a0 += dpp<XOR7>(a0);
    sc[g] = a0 * C2;
  }
  STAMP(4);
  u32x4 wz[8];
  {
    const int rq = lane >> 4, pc = lane & 15;
    const rsrc_t rO = mk(wo + (size_t)(h * DH + 24 * w) * D + 80 * s, 23 * D * 2 + 160), rD = mk(wd + (size_t)(48 * h + 6 * w) * D + 80 * s, 5 * D * 2 + 160);
    const int vo = pc < 10 ? rq * 6 * D * 2 + pc * 16 : 0x40000000, vd = pc < 10 ? rq * 2 * D * 2 + pc * 16 : 0x40000000;
#pragma unroll
    for (int i = 0; i < 6; ++i) wz[i] = ld16<AUX_NT>(rO, vo + i * D * 2, 0);
#pragma unroll
    for (int i = 0; i < 2; ++i) wz[6 + i] = ld16<AUX_NT>(rD, vd + i * D * 2, 0);
  }
  SB();
  float m = fmaxf(fmaxf(fmaxf(sc[0], sc[1]), fmaxf(sc[2], sc[3])), fmaxf(fmaxf(sc[4], sc[5]), fmaxf(sc[6], sc[7])));
  m = max_r8(m);
  float lsum = 0.f; unsigned pp[4];
#pragma unroll
  for (int g = 0; g < 4; ++g) {
    const float p0 = __builtin_amdgcn_exp2f(sc[2 * g] - m), p1 = __builtin_amdgcn_exp2f(sc[2 * g + 1] - m);
    lsum += p0 + p1; pp[g] = pack_bf16(p0, p1);
  }
  lsum = sum_r8(lsum);
#pragma unroll
  for (int j = 0; j < 24; ++j) acc[j] = 0.f;
  FMA_PAIR(kv + 0, kv + 3, pp[0]); SB();
  FMA_PAIR(kv + 6, kv + 9, pp[1]); SB();
  FMA_PAIR(kv + 12, kv + 15, pp[2]); SB();
  FMA_PAIR(kv + 18, kv + 21, pp[3]); SB();
  STAMP(5);
  reduce24_store(acc, sm.att[w], lane);
  if (lane == 0) { sm.att[w][192] = m; sm.att[w][193] = lsum; }
  LDS_BARRIER();
  if (tid < 97) {
    float mw[8], M = -INFINITY;
#pragma unroll
    for (int i = 0; i < 8; ++i) { mw[i] = sm.att[i][192]; M = fmaxf(M, mw[i]); }
    float o0 = 0.f, o1 = 0.f;
#pragma unroll
    for (int i = 0; i < 8; ++i) { const float e = __builtin_amdgcn_exp2f(mw[i] - M); const float2 t = *(const float2*)&sm.att[i][2 * tid]; o0 += e * t.x; o1 += e * t.y; }
    if (tid == 96) o0 = M;
    __builtin_amdgcn_raw_buffer_store_b128(u32x4{__float_as_uint(o0), tag, __float_as_uint(o1), tag}, rW, WS_R1 + b * REC1 + (tid == 96 ? 144 : tid) * 16, 0, AUX_SC1);
  }
  STAMP(6);
  {
    const int rec = WS_R1 + ((b & ~56) + r8 * 8) * REC1;
    const int p0 = 18 * w + c8, p2 = 18 * w + 16 + (c8 & 1);
    u32x4 a0, a1, a2, st;
    for (unsigned spins = 0; spins < SPIN_CAP; ++spins) {
      asm volatile("" ::: "memory");
      a0 = ld16<AUX_SC1>(rW, rec + p0 * 16, 0); a1 = ld16<AUX_SC1>(rW, rec + p0 * 16 + 128, 0); a2 = ld16<AUX_SC1>(rW, rec + p2 * 16, 0); st = ld16<AUX_SC1>(rW, rec + 144 * 16, 0);
      const bool ok = a0[1] == tag && a0[3] == tag && a1[1] == tag && a1[3] == tag && a2[1] == tag && a2[3] == tag && st[1] == tag && st[3] == tag;
      if (__all(ok)) break;
    }
    STAMP(7);
    const float ms = __uint_as_float(st[0]), M = max_r8(ms), e = __builtin_amdgcn_exp2f(ms - M), L = sum_r8(e * __uint_as_float(st[2]));
    const float so = e * gate / L;
    auto fin = [&](const u32x4& a, int p, bool wr_) {
      const float f = p < 96 ? so : 1.f;
      const float v0 = sum_r8(f * __uint_as_float(a[0])), v1 = sum_r8(f * __uint_as_float(a[2]));
      if (wr_) {
        if (p < 96) *(float2*)&sm.z[2 * p] = float2{v0, v1};
        else { const float u = 0.7978845608028654f * (v0 + 0.044715f * v0 * v0 * v0); const float th = 1.f - 2.f / (1.f + __expf(2.f * u));
               sm.z[96 + p] = (1.f - gate) * 0.5f * v0 * (1.f + th) * v1; }
      }
    };
    fin(a0, p0, r8 == 0); fin(a1, p0 + 8, r8 == 0); fin(a2, p2, lane < 2);
    if (tid < 16) sm.z[240 + tid] = 0.f;
  }
  LDS_BARRIER();
  {
    const int rq = lane >> 4;
    float zz[8], y[8];
#pragma unroll
    for (int i = 0; i < 8; i += 2) { const float2 t = *(const float2*)&sm.z[i < 6 ? 24 * w + rq * 6 + i : 192 + 6 * w + rq * 2]; zz[i] = t.x; zz[i + 1] = t.y; }
#pragma unroll
    for (int j = 0; j < 8; ++j) y[j] = 0.f;
#pragma unroll
    for (int i = 0; i < 8; ++i)
#pragma unroll
      for (int j = 0; j < 4; ++j) { y[2 * j] += zz[i] * bflo(wz[i][j]); y[2 * j + 1] += zz[i] * bfhi(wz[i][j]); }
    float y4[4];
#pragma unroll
    for (int j = 0; j < 4; ++j) y4[j] = tr32(y[j], y[j + 4]);
    const float ya = tr16(y4[0], y4[2]), yb = tr16(y4[1], y4[3]);
    if ((lane & 15) < 10) *(float2*)&sm.y[w][(lane & 15) * 8 + (lane >> 5) * 4 + ((lane >> 4) & 1) * 2] = float2{ya, yb};
  }
  LDS_BARRIER();
  if (w == 0) {
    if (lane < 40) {
      float o0 = 0.f, o1 = 0.f;
#pragma unroll
      for (int i = 0; i < 8; ++i) { const float2 t = *(const float2*)&sm.y[i][2 * lane]; o0 += t.x; o1 += t.y; }
      __builtin_amdgcn_raw_buffer_store_b128(u32x4{__float_as_uint(o0), tag, __float_as_uint(o1), tag}, rW, WS_R2 + (s * 32 + h) * REC2 + lane * 16, 0, AUX_SC1);
    }
    STAMP(8);
    if (h < 5) {
      const int rec = WS_R2 + (s * 32 + r8) * REC2 + h * 128 + c8 * 16;
      u32x4 a0, a1, a2, a3;
      for (unsigned spins = 0; spins < SPIN_CAP; ++spins) {
        asm volatile("" ::: "memory");
        a0 = ld16<AUX_SC1>(rW, rec, 0); a1 = ld16<AUX_SC1>(rW, rec, 8 * REC2); a2 = ld16<AUX_SC1>(rW, rec, 16 * REC2); a3 = ld16<AUX_SC1>(rW, rec, 24 * REC2);
        const bool ok = a0[1] == tag && a0[3] == tag && a1[1] == tag && a1[3] == tag && a2[1] == tag && a2[3] == tag && a3[1] == tag && a3[3] == tag;
        if (__all(ok)) break;
      }
      float v0 = (__uint_as_float(a0[0]) + __uint_as_float(a1[0])) + (__uint_as_float(a2[0]) + __uint_as_float(a3[0]));
      float v1 = (__uint_as_float(a0[2]) + __uint_as_float(a1[2])) + (__uint_as_float(a2[2]) + __uint_as_float(a3[2]));
      v0 = sum_r8(v0); v1 = sum_r8(v1);
      if (lane < 8) *(GAS unsigned*)(out + 80 * s + 16 * h + 2 * lane) = pack_bf16(v0, v1);
      if (b == 0 && lane == 0) __builtin_amdgcn_raw_buffer_store_b32(tag, rW, 0, 0, AUX_SC1);
      STAMP(9);
    }
  }
}

extern "C" void kernel_launch(void* const* d_in, const int* in_sizes, int n_in, void* d_out, int out_size, void* d_ws, size_t ws_size, hipStream_t stream) {
  for (int i = 0; i < n_in; ++i) fprintf(stderr, "PTR in%d %p\n", i, d_in[i]);
  fprintf(stderr, "PTR out %p ws %p %zu\n", d_out, d_ws, ws_size);
  { hipPointerAttribute_t a; if (hipPointerGetAttributes(&a, d_in[1]) == hipSuccess) fprintf(stderr, "ATTR type %d dev %d managed %d flags %u\n", (int)a.type, a.device, a.isManaged, a.allocationFlags);
    void* base; size_t sz; if (hipMemGetAddressRange((hipDeviceptr_t*)&base, &sz, d_in[1]) == hipSuccess) fprintf(stderr, "RANGE k %p %zu\n", base, sz);
    if (hipMemGetAddressRange((hipDeviceptr_t*)&base, &sz, d_in[3]) == hipSuccess) fprintf(stderr, "RANGE wq %p %zu\n", base, sz);
    if (hipMemGetAddressRange((hipDeviceptr_t*)&base, &sz, d_ws) == hipSuccess) fprintf(stderr, "RANGE ws %p %zu\n", base, sz); }
  fused_kernel<<<256, 512, 0, stream>>>((const unsigned short*)d_in[0], (const unsigned short*)d_in[1], (const unsigned short*)d_in[2], (const unsigned short*)d_in[3],
                                        (const unsigned short*)d_in[4], (const unsigned short*)d_in[6], (const unsigned short*)d_in[7], (const unsigned short*)d_in[8],
                                        (const unsigned short*)d_in[9], (unsigned short*)d_out, (unsigned char*)d_ws
  );
}
```

```cpp
#include <hip/hip_runtime.h>
#include <hip/hip_bf16.h>
#include <stdint.h>

typedef unsigned u32x4 __attribute__((ext_vector_type(4)));
typedef unsigned u32x2 __attribute__((ext_vector_type(2)));
typedef float f32x4 __attribute__((ext_vector_type(4)));
typedef __bf16 bf16x2 __attribute__((ext_vector_type(2)));
typedef __amdgpu_buffer_rsrc_t rsrc_t;
#define GAS __attribute__((address_space(1)))

constexpr int D = 640, H = 32, SK = 32768, DH = 192, W = 4096, F = 1536;
constexpr int NS = 8;
constexpr int AUX_NT = 2, AUX_SC1 = 16;
#define KV_AUX 0
constexpr int REC1 = 2560;
constexpr int WS_R1 = 4096, WS_R2 = WS_R1 + 256 * REC1, REC2 = 640, WS_END = WS_R2 + 256 * REC2;
constexpr unsigned SPIN_CAP = 1u << 20;

#define STAMP(i) do {} while (0)

__device__ __forceinline__ rsrc_t mk(const void* p, int bytes) { return __builtin_amdgcn_make_buffer_rsrc((void*)p, 0, bytes, 0x00020000); }
template <int AUX> __device__ __forceinline__ u32x4 ld16(rsrc_t r, int voff, int soff) { return __builtin_amdgcn_raw_buffer_load_b128(r, voff, soff, AUX); }
__device__ __forceinline__ float dot2(unsigned a, unsigned b, float c) {
  return __builtin_amdgcn_fdot2_f32_bf16(__builtin_bit_cast(bf16x2, a), __builtin_bit_cast(bf16x2, b), c, false);
}
__device__ __forceinline__ unsigned perm_lo(unsigned a, unsigned b) { return __builtin_amdgcn_perm(b, a, 0x05040100u); }
__device__ __forceinline__ unsigned perm_hi(unsigned a, unsigned b) { return __builtin_amdgcn_perm(b, a, 0x07060302u); }
__device__ __forceinline__ unsigned pack_bf16(float lo, float hi) { bf16x2 r = {(__bf16)lo, (__bf16)hi}; return __builtin_bit_cast(unsigned, r); }
__device__ __forceinline__ float bflo(unsigned u) { return __uint_as_float(u << 16); }
__device__ __forceinline__ float bfhi(unsigned u) { return __uint_as_float(u & 0xffff0000u); }
template <int CTRL> __device__ __forceinline__ float dpp(float x) {
  return __builtin_bit_cast(float, __builtin_amdgcn_mov_dpp(__builtin_bit_cast(int, x), CTRL, 0xf, 0xf, true));
}
constexpr int XOR1 = 0xB1, XOR2 = 0x4E, XOR7 = 0x141, XOR8 = 0x128;
__device__ __forceinline__ float tr32(float a, float b) { auto s = __builtin_amdgcn_permlane32_swap(__float_as_uint(a), __float_as_uint(b), false, false); return __uint_as_float(s[0]) + __uint_as_float(s[1]); }
__device__ __forceinline__ float tr16(float a, float b) { auto s = __builtin_amdgcn_permlane16_swap(__float_as_uint(a), __float_as_uint(b), false, false); return __uint_as_float(s[0]) + __uint_as_float(s[1]); }
__device__ __forceinline__ float max32(float a) { auto s = __builtin_amdgcn_permlane32_swap(__float_as_uint(a), __float_as_uint(a), false, false); return fmaxf(__uint_as_float(s[0]), __uint_as_float(s[1])); }
__device__ __forceinline__ float max16(float a) { auto s = __builtin_amdgcn_permlane16_swap(__float_as_uint(a), __float_as_uint(a), false, false); return fmaxf(__uint_as_float(s[0]), __uint_as_float(s[1])); }
__device__ __forceinline__ float sum_r8(float a) { a += dpp<XOR8>(a); a = tr16(a, a); return tr32(a, a); }
__device__ __forceinline__ float max_r8(float a) { a = fmaxf(a, dpp<XOR8>(a)); a = max16(a); return max32(a); }
#define SB() __builtin_amdgcn_sched_barrier(0)
#define LDS_BARRIER() do { asm volatile("s_waitcnt lgkmcnt(0)" ::: "memory"); __builtin_amdgcn_s_barrier(); asm volatile("" ::: "memory"); } while (0)

__device__ __forceinline__ void reduce24_store(float (&a)[24], float* dst, int lane) {
  float b[12], c[6];
#pragma unroll
  for (int i = 0; i < 12; ++i) b[i] = tr32(a[i], a[i + 12]);
#pragma unroll
  for (int i = 0; i < 6; ++i) c[i] = tr16(b[i], b[i + 6]);
  const bool hi = lane & 8;
  const int base = ((lane >> 5) & 1) * 12 + ((lane >> 4) & 1) * 6 + (hi ? 3 : 0);
#pragma unroll
  for (int i = 0; i < 3; ++i) {
    const float t1 = c[i] + dpp<XOR8>(c[i]), t2 = c[i + 3] + dpp<XOR8>(c[i + 3]);
    const int v = base + i;
    dst[(v >> 3) * 64 + (lane & 7) * 8 + (v & 7)] = hi ? t2 : t1;
  }
}

struct __attribute__((aligned(16))) Smem {
  float red[8][192];
  float att[8][200];
  float ffn[48][96];
  unsigned qb[8][96];
  float z[256];
  float y[8][80];
};

__global__ void __launch_bounds__(512, 2) fused_kernel(const unsigned short* __restrict__ x, const unsigned short* __restrict__ kc, const unsigned short* __restrict__ vc,
                                                      const unsigned short* __restrict__ wq, const unsigned short* __restrict__ wo, const unsigned short* __restrict__ wg,
                                                      const unsigned short* __restrict__ wu, const unsigned short* __restrict__ wd, const unsigned short* __restrict__ gw,
                                                      unsigned short* __restrict__ out, unsigned char* ws
) {
  __shared__ Smem sm;
  const int tid = threadIdx.x, lane = tid & 63, w = __builtin_amdgcn_readfirstlane(tid >> 6);
  const int b = blockIdx.x, h = (b & 7) * 4 + (b >> 6), s = (b >> 3) & 7;
  const int r8 = lane >> 3, c8 = lane & 7;
  STAMP(0);
  const size_t kvoff = ((size_t)h * SK + (SK - W) + s * 512 + w * 64) * DH;
  const rsrc_t rK = mk(kc + kvoff, 64 * DH * 2), rV = mk(vc + kvoff, 64 * DH * 2);
  const rsrc_t rQ = mk(wq + (size_t)(w * 80) * (H * DH) + h * DH, 80 * H * DH * 2 - h * DH * 2);
  const rsrc_t rW = mk(ws, WS_END);
  const int kvv = r8 * (DH * 2) + c8 * 16;
  const int qv = r8 * 10 * (H * DH * 2) + c8 * 16;

  u32x4 kv[24], wr[30];
#define LDK(g) _Pragma("unroll") for (int c = 0; c < 3; ++c) kv[(g) * 3 + c] = ld16<KV_AUX>(rK, kvv, (g) * 3072 + c * 128)
#define LDV(g) _Pragma("unroll") for (int c = 0; c < 3; ++c) kv[(g) * 3 + c] = ld16<KV_AUX>(rV, kvv, (g) * 3072 + c * 128)
#define LDQ(i) _Pragma("unroll") for (int c = 0; c < 3; ++c) wr[(i) * 3 + c] = ld16<0>(rQ, qv, (i) * (H * DH * 2) + c * 128)
  LDK(0); SB();
  const rsrc_t rX = mk(x, D * 2);
  const u32x4 xa = ld16<0>(rX, w * 160 + r8 * 20, 0);
  const unsigned xb = __builtin_amdgcn_raw_buffer_load_b32(rX, w * 160 + r8 * 20 + 16, 0, 0);
  const unsigned epoch = __builtin_amdgcn_raw_buffer_load_b32(rW, 0, 0, AUX_SC1);
  SB();
  LDQ(0); LDQ(1); LDQ(2); SB(); LDK(1); SB(); LDQ(3); LDQ(4); LDQ(5); SB(); LDK(2); SB(); LDQ(6); LDQ(7); LDQ(8); LDQ(9); SB(); LDK(3); SB();
  const int rg = lane / 12, mp = lane % 12;
  u32x4 fa, fb; unsigned xf;
  {
    const unsigned short* wm = mp < 6 ? wg : wu;
    const int row = 80 * s + 10 * w + 2 * (rg < 5 ? rg : 4);
    const GAS u32x4* p = (const GAS u32x4*)(wm + (size_t)row * F + 48 * h + (mp % 6) * 8);
    fa = p[0]; fb = p[F / 8];
    xf = __builtin_amdgcn_raw_buffer_load_b32(rX, row * 2, 0, 0);
  }
  const rsrc_t rG = mk(gw, D * 2);
  const u32x4 ga = ld16<0>(rG, lane * 20, 0), gxa = ld16<0>(rX, lane * 20, 0);
  const unsigned gb = __builtin_amdgcn_raw_buffer_load_b32(rG, lane * 20 + 16, 0, 0), gxb = __builtin_amdgcn_raw_buffer_load_b32(rX, lane * 20 + 16, 0, 0);
  SB();
  STAMP(1);
  float acc[24];
#pragma unroll
  for (int j = 0; j < 24; ++j) acc[j] = 0.f;
#define FMA_PAIR(A, B, P) _Pragma("unroll") for (int c = 0; c < 3; ++c) _Pragma("unroll") for (int j = 0; j < 4; ++j) { \
    acc[c * 8 + 2 * j] = dot2(perm_lo((A)[c][j], (B)[c][j]), P, acc[c * 8 + 2 * j]); \
    acc[c * 8 + 2 * j + 1] = dot2(perm_hi((A)[c][j], (B)[c][j]), P, acc[c * 8 + 2 * j + 1]); }
  FMA_PAIR(wr + 0, wr + 3, xa[0]); SB(); LDK(4); SB();
  FMA_PAIR(wr + 6, wr + 9, xa[1]); SB(); LDK(5); SB();
  FMA_PAIR(wr + 12, wr + 15, xa[2]); SB(); LDK(6); SB();
  FMA_PAIR(wr + 18, wr + 21, xa[3]); SB(); LDK(7); SB();
  FMA_PAIR(wr + 24, wr + 27, xb); SB();
  STAMP(2);
  reduce24_store(acc, sm.red[w], lane);
  {
    {
      f32x4 lo, hi;
#pragma unroll
      for (int j = 0; j < 2; ++j) {
        lo[2 * j] = dot2(perm_lo(fa[j], fb[j]), xf, 0.f); lo[2 * j + 1] = dot2(perm_hi(fa[j], fb[j]), xf, 0.f);
        hi[2 * j] = dot2(perm_lo(fa[j + 2], fb[j + 2]), xf, 0.f); hi[2 * j + 1] = dot2(perm_hi(fa[j + 2], fb[j + 2]), xf, 0.f);
      }
      f32x4* dst = (f32x4*)&sm.ffn[w * 6 + rg][mp * 8];
      dst[0] = lo; dst[1] = hi;
    }
  }
  float gate;
  {
    float t = dot2(gb, gxb, 0.f);
#pragma unroll
    for (int j = 0; j < 4; ++j) t = dot2(ga[j], gxa[j], t);
    t += dpp<XOR1>(t); t += dpp<XOR2>(t); t += dpp<XOR7>(t); t = sum_r8(t);
    gate = 1.f / (1.f + __expf(-t));
  }
  LDS_BARRIER();
  unsigned q[12];
  {
    if (lane < 48) {
      f32x4 t = *(const f32x4*)&sm.red[0][lane * 4];
#pragma unroll
      for (int i = 1; i < 8; ++i) t += *(const f32x4*)&sm.red[i][lane * 4];
      *(u32x2*)&sm.qb[w][lane * 2] = u32x2{pack_bf16(t[0], t[1]), pack_bf16(t[2], t[3])};
    }
    asm volatile("s_waitcnt lgkmcnt(0)" ::: "memory");
#pragma unroll
    for (int c = 0; c < 3; ++c) { const u32x4 t = *(const u32x4*)&sm.qb[w][c * 32 + c8 * 4]; q[c * 4] = t[0]; q[c * 4 + 1] = t[1]; q[c * 4 + 2] = t[2]; q[c * 4 + 3] = t[3]; }
  }
  const unsigned tag = epoch + 1u;
  if (w == 7 && lane < 48) {
    float g0 = 0.f, u0 = 0.f;
#pragma unroll 2
    for (int i = 0; i < 8; ++i)
#pragma unroll
      for (int k = 0; k < 5; ++k) { g0 += sm.ffn[i * 6 + k][lane]; u0 += sm.ffn[i * 6 + k][48 + lane]; }
    __builtin_amdgcn_raw_buffer_store_b128(u32x4{__float_as_uint(g0), tag, __float_as_uint(u0), tag}, rW, WS_R1 + b * REC1 + (96 + lane) * 16, 0, AUX_SC1);
  }
  STAMP(3);
  float sc[8];
  constexpr float C2 = 0.07216878364870322f * 1.4426950408889634f;
#pragma unroll
  for (int g = 0; g < 8; ++g) {
    float a0 = 0.f, a1 = 0.f;
#pragma unroll
    for (int c = 0; c < 3; ++c) { a0 = dot2(kv[g * 3 + c][0], q[c * 4], a0); a1 = dot2(kv[g * 3 + c][1], q[c * 4 + 1], a1); a0 = dot2(kv[g * 3 + c][2], q[c * 4 + 2], a0); a1 = dot2(kv[g * 3 + c][3], q[c * 4 + 3], a1); }
    SB(); LDV(g); SB();
    a0 += a1; a0 += dpp<XOR1>(a0); a0 += dpp<XOR2>(a0); a0 += dpp<XOR7>(a0);
    sc[g] = a0 * C2;
  }
  STAMP(4);
  u32x4 wz[8];
  {
    const int rq = lane >> 4, pc = lane & 15;
    const rsrc_t rO = mk(wo + (size_t)(h * DH + 24 * w) * D + 80 * s, 23 * D * 2 + 160), rD = mk(wd + (size_t)(48 * h + 6 * w) * D + 80 * s, 5 * D * 2 + 160);
    const int vo = pc < 10 ? rq * 6 * D * 2 + pc * 16 : 0x40000000, vd = pc < 10 ? rq * 2 * D * 2 + pc * 16 : 0x40000000;
#pragma unroll
    for (int i = 0; i < 6; ++i) wz[i] = ld16<0>(rO, vo + i * D * 2, 0);
#pragma unroll
    for (int i = 0; i < 2; ++i) wz[6 + i] = ld16<0>(rD, vd + i * D * 2, 0);
  }
  SB();
  float m = fmaxf(fmaxf(fmaxf(sc[0], sc[1]), fmaxf(sc[2], sc[3])), fmaxf(fmaxf(sc[4], sc[5]), fmaxf(sc[6], sc[7])));
  m = max_r8(m);
  float lsum = 0.f; unsigned pp[4];
#pragma unroll
  for (int g = 0; g < 4; ++g) {
    const float p0 = __builtin_amdgcn_exp2f(sc[2 * g] - m), p1 = __builtin_amdgcn_exp2f(sc[2 * g + 1] - m);
    lsum += p0 + p1; pp[g] = pack_bf16(p0, p1);
  }
  lsum = sum_r8(lsum);
#pragma unroll
  for (int j = 0; j < 24; ++j) acc[j] = 0.f;
  FMA_PAIR(kv + 0, kv + 3, pp[0]); SB();
  FMA_PAIR(kv + 6, kv + 9, pp[1]); SB();
  FMA_PAIR(kv + 12, kv + 15, pp[2]); SB();
  FMA_PAIR(kv + 18, kv + 21, pp[3]); SB();
  STAMP(5);
  reduce24_store(acc, sm.att[w], lane);
  if (lane == 0) { sm.att[w][192] = m; sm.att[w][193] = lsum; }
  LDS_BARRIER();
  if (tid < 97) {
    float mw[8], M = -INFINITY;
#pragma unroll
    for (int i = 0; i < 8; ++i) { mw[i] = sm.att[i][192]; M = fmaxf(M, mw[i]); }
    float o0 = 0.f, o1 = 0.f;
#pragma unroll
    for (int i = 0; i < 8; ++i) { const float e = __builtin_amdgcn_exp2f(mw[i] - M); const float2 t = *(const float2*)&sm.att[i][2 * tid]; o0 += e * t.x; o1 += e * t.y; }
    if (tid == 96) o0 = M;
    __builtin_amdgcn_raw_buffer_store_b128(u32x4{__float_as_uint(o0), tag, __float_as_uint(o1), tag}, rW, WS_R1 + b * REC1 + (tid == 96 ? 144 : tid) * 16, 0, AUX_SC1);
  }
  STAMP(6);
  {
    const int rec = WS_R1 + ((b & ~56) + r8 * 8) * REC1;
    const int p0 = 18 * w + c8, p2 = 18 * w + 16 + (c8 & 1);
    u32x4 a0, a1, a2, st;
    for (unsigned spins = 0; spins < SPIN_CAP; ++spins) {
      asm volatile("" ::: "memory");
      a0 = ld16<AUX_SC1>(rW, rec + p0 * 16, 0); a1 = ld16<AUX_SC1>(rW, rec + p0 * 16 + 128, 0); a2 = ld16<AUX_SC1>(rW, rec + p2 * 16, 0); st = ld16<AUX_SC1>(rW, rec + 144 * 16, 0);
      const bool ok = a0[1] == tag && a0[3] == tag && a1[1] == tag && a1[3] == tag && a2[1] == tag && a2[3] == tag && st[1] == tag && st[3] == tag;
      if (__all(ok)) break;
    }
    STAMP(7);
    const float ms = __uint_as_float(st[0]), M = max_r8(ms), e = __builtin_amdgcn_exp2f(ms - M), L = sum_r8(e * __uint_as_float(st[2]));
    const float so = e * gate / L;
    auto fin = [&](const u32x4& a, int p, bool wr_) {
      const float f = p < 96 ? so : 1.f;
      const float v0 = sum_r8(f * __uint_as_float(a[0])), v1 = sum_r8(f * __uint_as_float(a[2]));
      if (wr_) {
        if (p < 96) *(float2*)&sm.z[2 * p] = float2{v0, v1};
        else { const float u = 0.7978845608028654f * (v0 + 0.044715f * v0 * v0 * v0); const float th = 1.f - 2.f / (1.f + __expf(2.f * u));
               sm.z[96 + p] = (1.f - gate) * 0.5f * v0 * (1.f + th) * v1; }
      }
    };
    fin(a0, p0, r8 == 0); fin(a1, p0 + 8, r8 == 0); fin(a2, p2, lane < 2);
    if (tid < 16) sm.z[240 + tid] = 0.f;
  }
  LDS_BARRIER();
  {
    const int rq = lane >> 4;
    float zz[8], y[8];
#pragma unroll
    for (int i = 0; i < 8; i += 2) { const float2 t = *(const float2*)&sm.z[i < 6 ? 24 * w + rq * 6 + i : 192 + 6 * w + rq * 2]; zz[i] = t.x; zz[i + 1] = t.y; }
#pragma unroll
    for (int j = 0; j < 8; ++j) y[j] = 0.f;
#pragma unroll
    for (int i = 0; i < 8; ++i)
#pragma unroll
      for (int j = 0; j < 4; ++j) { y[2 * j] += zz[i] * bflo(wz[i][j]); y[2 * j + 1] += zz[i] * bfhi(wz[i][j]); }
    float y4[4];
#pragma unroll
    for (int j = 0; j < 4; ++j) y4[j] = tr32(y[j], y[j + 4]);
    const float ya = tr16(y4[0], y4[2]), yb = tr16(y4[1], y4[3]);
    if ((lane & 15) < 10) *(float2*)&sm.y[w][(lane & 15) * 8 + (lane >> 5) * 4 + ((lane >> 4) & 1) * 2] = float2{ya, yb};
  }
  LDS_BARRIER();
  if (w == 0) {
    if (lane < 40) {
      float o0 = 0.f, o1 = 0.f;
#pragma unroll
      for (int i = 0; i < 8; ++i) { const float2 t = *(const float2*)&sm.y[i][2 * lane]; o0 += t.x; o1 += t.y; }
      __builtin_amdgcn_raw_buffer_store_b128(u32x4{__float_as_uint(o0), tag, __float_as_uint(o1), tag}, rW, WS_R2 + (s * 32 + h) * REC2 + lane * 16, 0, AUX_SC1);
    }
    STAMP(8);
    if (h < 5) {
      const int rec = WS_R2 + (s * 32 + r8) * REC2 + h * 128 + c8 * 16;
      u32x4 a0, a1, a2, a3;
      for (unsigned spins = 0; spins < SPIN_CAP; ++spins) {
        asm volatile("" ::: "memory");
        a0 = ld16<AUX_SC1>(rW, rec, 0); a1 = ld16<AUX_SC1>(rW, rec, 8 * REC2); a2 = ld16<AUX_SC1>(rW, rec, 16 * REC2); a3 = ld16<AUX_SC1>(rW, rec, 24 * REC2);
        const bool ok = a0[1] == tag && a0[3] == tag && a1[1] == tag && a1[3] == tag && a2[1] == tag && a2[3] == tag && a3[1] == tag && a3[3] == tag;
        if (__all(ok)) break;
      }
      float v0 = (__uint_as_float(a0[0]) + __uint_as_float(a1[0])) + (__uint_as_float(a2[0]) + __uint_as_float(a3[0]));
      float v1 = (__uint_as_float(a0[2]) + __uint_as_float(a1[2])) + (__uint_as_float(a2[2]) + __uint_as_float(a3[2]));
      v0 = sum_r8(v0); v1 = sum_r8(v1);
      if (lane < 8) *(GAS unsigned*)(out + 80 * s + 16 * h + 2 * lane) = pack_bf16(v0, v1);
      if (b == 0 && lane == 0) __builtin_amdgcn_raw_buffer_store_b32(tag, rW, 0, 0, AUX_SC1);
      STAMP(9);
    }
  }
}

extern "C" void kernel_launch(void* const* d_in, const int* in_sizes, int n_in, void* d_out, int out_size, void* d_ws, size_t ws_size, hipStream_t stream) {
  fused_kernel<<<256, 512, 0, stream>>>((const unsigned short*)d_in[0], (const unsigned short*)d_in[1], (const unsigned short*)d_in[2], (const unsigned short*)d_in[3],
                                        (const unsigned short*)d_in[4], (const unsigned short*)d_in[6], (const unsigned short*)d_in[7], (const unsigned short*)d_in[8],
                                        (const unsigned short*)d_in[9], (unsigned short*)d_out, (unsigned char*)d_ws
  );
}
```

```cpp
#include <hip/hip_runtime.h>
#include <hip/hip_bf16.h>
#include <stdint.h>

typedef unsigned u32x4 __attribute__((ext_vector_type(4)));
typedef unsigned u32x2 __attribute__((ext_vector_type(2)));
typedef float f32x2 __attribute__((ext_vector_type(2)));
typedef __bf16 bf16x2 __attribute__((ext_vector_type(2)));
typedef __amdgpu_buffer_rsrc_t rsrc_t;
#define GAS __attribute__((address_space(1)))
#define LAS __attribute__((address_space(3)))

constexpr int D = 640, H = 32, SK = 32768, DH = 192, W = 4096, F = 1536;
constexpr int AUX_NT = 2, AUX_SC1 = 16;
#define KV_AUX AUX_NT
#define PW_AUX AUX_NT
#define ZW_AUX AUX_NT
constexpr int REC0 = 1536, RECF = 512, REC1 = 1664, REC2 = 512;
constexpr int WS_R0 = 4096, WS_RF = WS_R0 + 256 * REC0, WS_R1 = WS_RF + 240 * RECF, WS_R2 = WS_R1 + 256 * REC1, WS_END = WS_R2 + 240 * REC2;
#define PSLEEP 0
#define POLL_SLEEP() do { if (PSLEEP) __builtin_amdgcn_s_sleep(PSLEEP); } while (0)
constexpr unsigned SPIN_CAP = 1u << 20;
constexpr int OOB = 0x40000000;

#define SB() __builtin_amdgcn_sched_barrier(0)
#define STAMP(i) do {} while (0)
#define STAMP_OUT() do {} while (0)

__device__ __forceinline__ rsrc_t mk(const void* p, int bytes) { return __builtin_amdgcn_make_buffer_rsrc((void*)p, 0, bytes, 0x00020000); }
template <int AUX> __device__ __forceinline__ u32x4 ld16(rsrc_t r, int voff, int soff) { return __builtin_amdgcn_raw_buffer_load_b128(r, voff, soff, AUX); }
__device__ __forceinline__ void st_unit(rsrc_t r, int voff, float a, float b, unsigned tag) {
  __builtin_amdgcn_raw_buffer_store_b128(u32x4{__float_as_uint(a), tag, __float_as_uint(b), tag}, r, voff, 0, AUX_SC1);
}
template <int N> __device__ __forceinline__ bool missing(const u32x4* a, unsigned t) {
  unsigned r = __builtin_amdgcn_bitop3_b32(a[0][1], a[0][3], t, 0x7e);
#pragma unroll
  for (int i = 1; i < N; ++i) r |= __builtin_amdgcn_bitop3_b32(a[i][1], a[i][3], t, 0x7e);
  return r != 0;
}
__device__ __forceinline__ float dot2(unsigned a, unsigned b, float c) {
  return __builtin_amdgcn_fdot2_f32_bf16(__builtin_bit_cast(bf16x2, a), __builtin_bit_cast(bf16x2, b), c, false);
}
__device__ __forceinline__ unsigned perm_lo(unsigned a, unsigned b) { return __builtin_amdgcn_perm(b, a, 0x05040100u); }
__device__ __forceinline__ unsigned perm_hi(unsigned a, unsigned b) { return __builtin_amdgcn_perm(b, a, 0x07060302u); }
__device__ __forceinline__ unsigned pack_bf16(float lo, float hi) { bf16x2 r = {(__bf16)lo, (__bf16)hi}; return __builtin_bit_cast(unsigned, r); }
__device__ __forceinline__ float f(unsigned u) { return __uint_as_float(u); }
template <int CTRL> __device__ __forceinline__ float dpp(float x) {
  return __builtin_bit_cast(float, __builtin_amdgcn_mov_dpp(__builtin_bit_cast(int, x), CTRL, 0xf, 0xf, true));
}
constexpr int XOR1 = 0xB1, XOR2 = 0x4E, XOR7 = 0x141, XOR8 = 0x128;
__device__ __forceinline__ float tr32(float a, float b) { auto s = __builtin_amdgcn_permlane32_swap(__float_as_uint(a), __float_as_uint(b), false, false); return f(s[0]) + f(s[1]); }
__device__ __forceinline__ float tr16(float a, float b) { auto s = __builtin_amdgcn_permlane16_swap(__float_as_uint(a), __float_as_uint(b), false, false); return f(s[0]) + f(s[1]); }
__device__ __forceinline__ float max32(float a) { auto s = __builtin_amdgcn_permlane32_swap(__float_as_uint(a), __float_as_uint(a), false, false); return fmaxf(f(s[0]), f(s[1])); }
__device__ __forceinline__ float max16(float a) { auto s = __builtin_amdgcn_permlane16_swap(__float_as_uint(a), __float_as_uint(a), false, false); return fmaxf(f(s[0]), f(s[1])); }
__device__ __forceinline__ float add8(float a) { return a + dpp<XOR8>(a); }
__device__ __forceinline__ float sum_r8(float a) { a = add8(a); a = tr16(a, a); return tr32(a, a); }
__device__ __forceinline__ float max_r8(float a) { a = fmaxf(a, dpp<XOR8>(a)); a = max16(a); return max32(a); }
#define LDS_WAIT() asm volatile("s_waitcnt lgkmcnt(0)" ::: "memory")
#define LDS_BARRIER() do { LDS_WAIT(); __builtin_amdgcn_s_barrier(); asm volatile("" ::: "memory"); SB(); } while (0)
__device__ __forceinline__ void pair8(float* acc, const u32x4& A, const u32x4& B, unsigned P) {
#pragma unroll
  for (int j = 0; j < 4; ++j) { acc[2 * j] = dot2(perm_lo(A[j], B[j]), P, acc[2 * j]); acc[2 * j + 1] = dot2(perm_hi(A[j], B[j]), P, acc[2 * j + 1]); }
}

struct __attribute__((aligned(16))) Smem {
  float att[8][192];
  float y[4][64];
  unsigned qb[96];
  unsigned zb[4][32];
  unsigned hb[64];
  unsigned qf[4];
  unsigned vf[4];
  float ml[16];
  float g;
};

__global__ void __launch_bounds__(512, 2) fused_kernel(const unsigned short* __restrict__ x, const unsigned short* __restrict__ kc, const unsigned short* __restrict__ vc,
                                                      const unsigned short* __restrict__ wq, const unsigned short* __restrict__ wo, const unsigned short* __restrict__ wg,
                                                      const unsigned short* __restrict__ wu, const unsigned short* __restrict__ wd, const unsigned short* __restrict__ gw,
                                                      unsigned short* __restrict__ out, unsigned char* ws
) {
  __shared__ Smem sm;
  const int tid = threadIdx.x, lane = tid & 63, w = __builtin_amdgcn_readfirstlane(tid >> 6);
  const int b = blockIdx.x, x7 = b & 7, j = b >> 3, h = x7 * 4 + (j >> 3), s = j & 7;
#define PXO 4
  const int g3 = j / 10, c = j - 10 * g3, px = (x7 + PXO) & 7, G = 3 * px + g3;
  const bool proj = j < 30;
  const int r8 = lane >> 3, c8 = lane & 7, b4 = (lane >> 4) & 1, b5 = lane >> 5;
  STAMP(0);
  asm volatile("" :: "s"(x), "s"(kc), "s"(vc), "s"(wq), "s"(wo), "s"(wg), "s"(wu), "s"(wd), "s"(gw), "s"(out), "s"(ws));
  const rsrc_t rW = mk(ws, WS_END), rX = mk(x, D * 2);
  unsigned epoch;
  if (w < 4) {
    const int nr = w < 3 ? 10 : 16, stride = w < 3 ? H * DH * 2 : F * 2, row0 = w < 3 ? 80 * s : 128 * (c < 5 ? c : c - 5);
    const unsigned short* pb = w < 3 ? wq + h * DH + w * 64 : (c < 5 ? wg : wu) + 64 * G;
    const bool on = w < 3 || proj;
    const rsrc_t rP = mk(pb, on ? 639 * stride + 128 : 0);
    const int pv = (row0 + r8 * nr) * stride + c8 * 16, xv = (row0 + r8 * nr) * 2;
    u32x4 wr[16], xc;
#pragma unroll
    for (int i = 0; i < 10; ++i) wr[i] = ld16<PW_AUX>(rP, pv, i * stride);
    const u32x4 xa = ld16<0>(rX, xv, 0);
    const unsigned xb = __builtin_amdgcn_raw_buffer_load_b32(rX, xv + 16, 0, 0);
    epoch = __builtin_amdgcn_raw_buffer_load_b32(rW, 0, 0, AUX_SC1);
    if (w == 3) {
#pragma unroll
      for (int i = 10; i < 16; ++i) wr[i] = ld16<PW_AUX>(rP, pv, i * F * 2);
      xc = ld16<0>(rX, xv + 16, 0);
    }
    SB();
    if (lane < 24) sm.qf[lane] = 0;
    LDS_BARRIER();
    float a[8] = {};
#pragma unroll
    for (int i = 0; i < 4; ++i) pair8(a, wr[2 * i], wr[2 * i + 1], xa[i]);
    pair8(a, wr[8], wr[9], xb);
    if (w == 3) {
#pragma unroll
      for (int i = 1; i < 4; ++i) pair8(a, wr[8 + 2 * i], wr[9 + 2 * i], xc[i]);
    }
    float t[4];
#pragma unroll
    for (int i = 0; i < 4; ++i) t[i] = tr32(a[i], a[i + 4]);
    st_unit(rW, on && !(lane & 8) ? (w < 3 ? WS_R0 + b * REC0 + w * 512 : WS_RF + (G * 10 + c) * RECF) + (c8 * 4 + b5 * 2 + b4) * 16 : OOB, add8(tr16(t[0], t[2])), add8(tr16(t[1], t[3])), epoch + 1u);
  } else {
    epoch = __builtin_amdgcn_raw_buffer_load_b32(rW, 0, 0, AUX_SC1);
    __builtin_amdgcn_s_barrier();
  }
  SB();
  STAMP(1);
  const size_t kvoff = ((size_t)h * SK + (SK - W) + s * 512 + w * 64) * DH;
  const rsrc_t rK = mk(kc + kvoff, 64 * DH * 2), rV = mk(vc + kvoff, 64 * DH * 2);
  const int kvv = r8 * (DH * 2) + c8 * 16;
  u32x4 k[24], v[24], gq[4];
#pragma unroll
  for (int i = 0; i < 24; ++i) k[i] = ld16<KV_AUX>(rK, kvv, (i / 3) * 3072 + (i % 3) * 128);
  SB();
  STAMP(2);
  const int qo = w < 3 ? WS_R0 + ((b & ~56) + r8 * 8) * REC0 + (w * 32 + c8) * 16 : OOB;
#pragma unroll
  for (int i = 0; i < 4; ++i) gq[i] = ld16<AUX_SC1>(rW, qo, i * 128);
  SB();
#pragma unroll
  for (int i = 0; i < 24; ++i) v[i] = ld16<KV_AUX>(rV, kvv, ((i / 6) * 2 + (i & 1)) * 3072 + (i % 6 / 2) * 128);
  SB();
  if (w >= 4 && lane == 0) *(volatile LAS unsigned*)&sm.vf[w - 4] = 1u;
  STAMP(3);
  const unsigned tag = epoch + 1u;
  if (w < 3) {
    if (__any(missing<4>(gq, tag))) {
#pragma unroll 1
      for (unsigned spins = 0; spins < SPIN_CAP; ++spins) {
        asm volatile("" ::: "memory");
#pragma unroll
        for (int i = 0; i < 4; ++i) gq[i] = ld16<AUX_SC1>(rW, qo, i * 128);
        if (!__any(missing<4>(gq, tag))) break;
      }
    }
    const float t0 = tr32(f(gq[0][0]), f(gq[2][0])), t1 = tr32(f(gq[0][2]), f(gq[2][2])), t2 = tr32(f(gq[1][0]), f(gq[3][0])), t3 = tr32(f(gq[1][2]), f(gq[3][2]));
    sm.qb[w * 32 + (b5 * 2 + b4) * 8 + c8] = pack_bf16(add8(tr16(t0, t2)), add8(tr16(t1, t3)));
    asm volatile("" ::: "memory");
    if (lane == 0) *(volatile LAS unsigned*)&sm.qf[w] = 1u;
  }
#pragma unroll 1
  for (unsigned spins = 0; spins < SPIN_CAP; ++spins) {
    const u32x4 fl = *(volatile LAS u32x4*)sm.qf;
    if (__builtin_amdgcn_readfirstlane(fl[0] & fl[1] & fl[2])) break;
    __builtin_amdgcn_s_sleep(1);
  }
  asm volatile("" ::: "memory");
  unsigned q[12];
#pragma unroll
  for (int i = 0; i < 3; ++i) { const u32x4 t = *(const u32x4*)&sm.qb[i * 32 + c8 * 4]; q[i * 4] = t[0]; q[i * 4 + 1] = t[1]; q[i * 4 + 2] = t[2]; q[i * 4 + 3] = t[3]; }
  STAMP(5);
  float sc[8];
  constexpr float C2 = 0.07216878364870322f * 1.4426950408889634f;
#pragma unroll
  for (int g = 0; g < 8; ++g) {
    float a0 = 0.f, a1 = 0.f;
#pragma unroll
    for (int i = 0; i < 3; ++i) { a0 = dot2(k[g * 3 + i][0], q[i * 4], a0); a1 = dot2(k[g * 3 + i][1], q[i * 4 + 1], a1); a0 = dot2(k[g * 3 + i][2], q[i * 4 + 2], a0); a1 = dot2(k[g * 3 + i][3], q[i * 4 + 3], a1); }
    a0 += a1; a0 += dpp<XOR1>(a0); a0 += dpp<XOR2>(a0); a0 += dpp<XOR7>(a0);
    sc[g] = a0 * C2;
  }
  float m = fmaxf(fmaxf(fmaxf(sc[0], sc[1]), fmaxf(sc[2], sc[3])), fmaxf(fmaxf(sc[4], sc[5]), fmaxf(sc[6], sc[7])));
  m = max_r8(m);
  float lsum = 0.f; unsigned pp[4];
#pragma unroll
  for (int g = 0; g < 4; ++g) {
    const float p0 = __builtin_amdgcn_exp2f(sc[2 * g] - m), p1 = __builtin_amdgcn_exp2f(sc[2 * g + 1] - m);
    lsum += p0 + p1; pp[g] = pack_bf16(p0, p1);
  }
  lsum = sum_r8(lsum);
  if (lane == 0) *(volatile LAS u32x2*)&sm.ml[2 * w] = u32x2{__float_as_uint(m), __float_as_uint(lsum)};
  {
    float acc[24] = {};
#pragma unroll
    for (int g = 0; g < 3; ++g) {
#pragma unroll
      for (int i = 0; i < 3; ++i) pair8(acc + i * 8, v[6 * g + 2 * i], v[6 * g + 2 * i + 1], pp[g]);
#pragma unroll
      for (int i = 0; i < 24; ++i) asm volatile("" : "+v"(acc[i]));
      SB();
    }
#pragma unroll
    for (int i = 0; i < 3; ++i) {
      pair8(acc + i * 8, v[18 + 2 * i], v[19 + 2 * i], pp[3]);
      float t[4];
#pragma unroll
      for (int e = 0; e < 4; ++e) t[e] = tr32(acc[i * 8 + e], acc[i * 8 + e + 4]);
      const float v0 = add8(tr16(t[0], t[2])), v1 = add8(tr16(t[1], t[3]));
      *(float2*)&sm.att[w][i * 64 + c8 * 8 + b5 * 4 + b4 * 2] = float2{v0, v1};
      SB();
    }
  }
  STAMP(7);
  if (w < 4) {
    u32x4 zd[2], zo[8];
    if (proj) {
      if (w == 0) {
        const int ho = WS_RF + (G * 10 + 5 * b5) * RECF + (lane & 31) * 16;
        u32x4 gh[5];
#pragma unroll 1
        for (unsigned spins = 0; spins < SPIN_CAP; ++spins) {
          asm volatile("" ::: "memory");
#pragma unroll
          for (int i = 0; i < 5; ++i) gh[i] = ld16<AUX_SC1>(rW, ho, i * RECF);
          if (!__any(missing<5>(gh, tag))) break;
        }
        const float s0 = (f(gh[0][0]) + f(gh[1][0])) + (f(gh[2][0]) + f(gh[3][0])) + f(gh[4][0]), s1 = (f(gh[0][2]) + f(gh[1][2])) + (f(gh[2][2]) + f(gh[3][2])) + f(gh[4][2]);
        const auto p0 = __builtin_amdgcn_permlane32_swap(__float_as_uint(s0), __float_as_uint(s0), false, false), p1 = __builtin_amdgcn_permlane32_swap(__float_as_uint(s1), __float_as_uint(s1), false, false);
        auto act = [](float a, float u) { const float z = 0.7978845608028654f * (a + 0.044715f * a * a * a); return 0.5f * a * (2.f - 2.f / (1.f + __expf(2.f * z))) * u; };
        sm.hb[lane] = pack_bf16(act(f(p0[0]), f(p0[1])), act(f(p1[0]), f(p1[1])));
      } else if (w == 1) {
        const rsrc_t rG = mk(gw, D * 2);
        const u32x4 g0 = ld16<0>(rG, lane * 16, 0), g1 = ld16<0>(rX, lane * 16, 0), g2 = ld16<0>(rG, 1024 + lane * 16, 0), g3_ = ld16<0>(rX, 1024 + lane * 16, 0);
        float t = 0.f;
#pragma unroll
        for (int i = 0; i < 4; ++i) { t = dot2(g0[i], g1[i], t); t = dot2(g2[i], g3_[i], t); }
        t += dpp<XOR1>(t); t += dpp<XOR2>(t); t += dpp<XOR7>(t); t = sum_r8(t);
        if (lane == 0) sm.g = 1.f / (1.f + __expf(-t));
      }
#pragma unroll 1
      for (unsigned spins = 0; spins < SPIN_CAP; ++spins) {
        if (__builtin_amdgcn_readfirstlane(*(volatile LAS unsigned*)&sm.vf[w])) break;
        __builtin_amdgcn_s_sleep(1);
      }
      const rsrc_t rO = mk(wo + (size_t)(256 * G + 64 * w) * D + 64 * c, 63 * D * 2 + 128), rD = mk(wd + (size_t)(64 * G + 16 * w) * D + 64 * c, 15 * D * 2 + 128);
#pragma unroll
      for (int i = 0; i < 2; ++i) zd[i] = ld16<ZW_AUX>(rD, r8 * 2 * D * 2 + c8 * 16, i * D * 2);
#pragma unroll
      for (int i = 0; i < 8; ++i) zo[i] = ld16<ZW_AUX>(rO, r8 * 8 * D * 2 + c8 * 16, i * D * 2);
      SB();
    }
    STAMP(8);
    float es[4];
    {
      u32x2 t;
#pragma unroll 1
      for (unsigned spins = 0; spins < SPIN_CAP; ++spins) {
        t = *(volatile LAS u32x2*)&sm.ml[2 * c8];
        if (!__any(t[1] == 0)) break;
        __builtin_amdgcn_s_sleep(1);
      }
      float M = f(t[0]);
      M = fmaxf(M, dpp<XOR1>(M)); M = fmaxf(M, dpp<XOR2>(M)); M = fmaxf(M, dpp<XOR7>(M));
      const float e = __builtin_amdgcn_exp2f(f(t[0]) - M);
      float L = e * f(t[1]);
      L += dpp<XOR1>(L); L += dpp<XOR2>(L); L += dpp<XOR7>(L);
      if (w == 0) st_unit(rW, lane == 0 ? WS_R1 + b * REC1 + 96 * 16 : OOB, M, L, tag);
#pragma unroll
      for (int i = 0; i < 4; ++i) { es[i] = b5 ? f(__builtin_amdgcn_readlane(__float_as_uint(e), 4 + i)) : f(__builtin_amdgcn_readlane(__float_as_uint(e), i)); asm volatile("" : "+v"(es[i])); }
    }
    const int l5 = lane & 31, mp = 24 * w + (l5 < 24 ? l5 : 0);
    int mo = lane < 24 ? WS_R1 + b * REC1 + mp * 16 : OOB;
    const LAS float* ma = (const LAS float*)&sm.att[4 * b5][2 * mp];
    const int ci = 4 * g3 + w, hl = ci / 3;
    int rec = WS_R1 + (px + 8 * (8 * hl + r8)) * REC1, ro = rec + (32 * (ci - 3 * hl) + c8) * 16;
    asm volatile("" : "+v"(mo), "+v"(ma), "+v"(rec), "+v"(ro));
    STAMP(4);
    LDS_BARRIER();
    STAMP(9);
    {
      f32x2 t[4];
#pragma unroll
      for (int i = 0; i < 4; ++i) t[i] = *(const LAS f32x2*)(ma + 192 * i);
      SB();
      const f32x2 a = (t[0] * es[0] + t[1] * es[1]) + (t[2] * es[2] + t[3] * es[3]);
      st_unit(rW, mo, tr32(a[0], a[0]), tr32(a[1], a[1]), tag);
    }
    SB();
    STAMP(6);
    if (proj) {
      u32x4 a[4], st;
      st = ld16<AUX_SC1>(rW, rec, 96 * 16);
#pragma unroll
      for (int i = 0; i < 4; ++i) a[i] = ld16<AUX_SC1>(rW, ro, i * 128);
      SB();
      STAMP(10);
      const float gate = sm.g;
      float y[8] = {};
      pair8(y, zd[0], zd[1], sm.hb[8 * w + r8]);
#pragma unroll
      for (int i = 0; i < 8; ++i) { y[i] *= 1.f - gate; asm volatile("" : "+v"(y[i])); }
      unsigned zp[4][8];
#pragma unroll
      for (int i = 0; i < 4; ++i)
#pragma unroll
        for (int e = 0; e < 4; ++e) { zp[i][2 * e] = perm_lo(zo[2 * i][e], zo[2 * i + 1][e]); zp[i][2 * e + 1] = perm_hi(zo[2 * i][e], zo[2 * i + 1][e]); asm volatile("" : "+v"(zp[i][2 * e]), "+v"(zp[i][2 * e + 1])); }
      SB();
      STAMP(11);
#pragma unroll 1
      for (unsigned spins = 0; spins < SPIN_CAP && missing<1>(&st, tag); ++spins) {
        asm volatile("" ::: "memory");
        st = ld16<AUX_SC1>(rW, rec, 96 * 16);
      }
      float ws_;
      {
        const float ms = f(st[0]), e = __builtin_amdgcn_exp2f(ms - max_r8(ms));
        ws_ = e * gate * __builtin_amdgcn_rcpf(sum_r8(e * f(st[2])));
      }
      LAS unsigned* zw = (LAS unsigned*)&sm.zb[w][(b5 * 2 + b4) * 8 + c8];
      const LAS u32x4* zr = (const LAS u32x4*)&sm.zb[w][r8 * 4];
      LAS f32x2* yw = (LAS f32x2*)&sm.y[w][c8 * 8 + b5 * 4 + b4 * 2];
      const LAS f32x2* yr = (const LAS f32x2*)&sm.y[2 * b5][2 * l5];
      int yo = lane < 32 ? WS_R2 + (c * 24 + G) * REC2 + lane * 16 : OOB;
      asm volatile("" : "+v"(ws_), "+v"(zw), "+v"(zr), "+v"(yw), "+v"(yr), "+v"(yo));
      SB();
      STAMP(15);
#pragma unroll 1
      for (unsigned spins = 0; spins < SPIN_CAP && missing<4>(a, tag); ++spins) {
        asm volatile("" ::: "memory");
        POLL_SLEEP();
#pragma unroll
        for (int i = 0; i < 4; ++i) a[i] = ld16<AUX_SC1>(rW, ro, i * 128);
      }
      STAMP(12);
      {
        float u[8];
#pragma unroll
        for (int i = 0; i < 8; ++i) u[i] = ws_ * f(a[i >> 1][(i & 1) * 2]);
        SB();
        const float t0 = tr32(u[0], u[4]), t1 = tr32(u[1], u[5]), t2 = tr32(u[2], u[6]), t3 = tr32(u[3], u[7]);
        *zw = pack_bf16(add8(tr16(t0, t2)), add8(tr16(t1, t3)));
      }
      asm volatile("" ::: "memory");
      {
        const u32x4 za = *zr;
#pragma unroll
        for (int i = 0; i < 4; ++i)
#pragma unroll
          for (int e = 0; e < 8; ++e) y[e] = dot2(zp[i][e], za[i], y[e]);
        float t[4];
#pragma unroll
        for (int e = 0; e < 4; ++e) t[e] = tr32(y[e], y[e + 4]);
        *yw = f32x2{add8(tr16(t[0], t[2])), add8(tr16(t[1], t[3]))};
      }
      LDS_BARRIER();
      if (w == 0) {
        const f32x2 t = yr[0] + yr[32];
        st_unit(rW, yo, tr32(t[0], t[0]), tr32(t[1], t[1]), tag);
      }
      STAMP(13);
    }
  } else {
    LDS_BARRIER();
    if (j == 30 || (j == 31 && (x7 & 3) == 0)) {
      const int rc = (j - 30) * 8 + 2 * (x7 & 3) + (x7 >> 2);
      const int rec = WS_R2 + (rc * 24 + r8) * REC2 + ((w - 4) * 8 + c8) * 16;
      const rsrc_t rY = mk(out, D * 2);
      int oo = (lane & 24) == 0 ? (64 * rc + 16 * (w - 4) + 2 * c8 + b5) * 2 : OOB;
      asm volatile("" : "+v"(oo));
      u32x4 a[3];
#pragma unroll 1
      for (unsigned spins = 0; spins < SPIN_CAP; ++spins) {
        asm volatile("" ::: "memory");
#pragma unroll
        for (int i = 0; i < 3; ++i) a[i] = ld16<AUX_SC1>(rW, rec, i * 8 * REC2);
        if (!missing<3>(a, tag)) break;
      }
      float v0 = tr32(f(a[0][0]) + f(a[1][0]) + f(a[2][0]), f(a[0][2]) + f(a[1][2]) + f(a[2][2]));
      v0 = add8(tr16(v0, v0));
      __builtin_amdgcn_raw_buffer_store_b16((unsigned short)pack_bf16(v0, v0), rY, oo, 0, 0);
      if (b == 240 && tid == 256) __builtin_amdgcn_raw_buffer_store_b32(tag, rW, 0, 0, AUX_SC1);
      STAMP(14);
    }
  }
  STAMP_OUT();
}

extern "C" void kernel_launch(void* const* d_in, const int* in_sizes, int n_in, void* d_out, int out_size, void* d_ws, size_t ws_size, hipStream_t stream) {
  fused_kernel<<<256, 512, 0, stream>>>((const unsigned short*)d_in[0], (const unsigned short*)d_in[1], (const unsigned short*)d_in[2], (const unsigned short*)d_in[3],
                                        (const unsigned short*)d_in[4], (const unsigned short*)d_in[6], (const unsigned short*)d_in[7], (const unsigned short*)d_in[8],
                                        (const unsigned short*)d_in[9], (unsigned short*)d_out, (unsigned char*)d_ws
  );
}
```

```cpp
#include <hip/hip_runtime.h>
#include <hip/hip_bf16.h>
#include <stdint.h>

typedef unsigned u32x4 __attribute__((ext_vector_type(4)));
typedef unsigned u32x2 __attribute__((ext_vector_type(2)));
typedef float f32x2 __attribute__((ext_vector_type(2)));
typedef __bf16 bf16x2 __attribute__((ext_vector_type(2)));
typedef __amdgpu_buffer_rsrc_t rsrc_t;
#define GAS __attribute__((address_space(1)))
#define LAS __attribute__((address_space(3)))

constexpr int D = 640, H = 32, SK = 32768, DH = 192, W = 4096, F = 1536;
constexpr int AUX_NT = 2, AUX_SC1 = 16;
constexpr int ASTR = 64;
constexpr int PFWG = 16;
constexpr int MS = 8, MW = 64, MG = 512;
constexpr int DT = 284, TOFF = 1700, BU = 3, PP0 = 6, WIN = 4, WMAX = 5;
constexpr int REC0 = 1536, RECF = 512, REC1 = 1664, REC2 = 512;
constexpr int WS_R0 = 4096, WS_RF = WS_R0 + 256 * REC0, WS_R1 = WS_RF + 240 * RECF, WS_R2 = WS_R1 + 256 * REC1, WS_ACC = WS_R2 + 240 * REC2, WS_FL = WS_ACC + 80 * ASTR, WS_END = WS_FL + 10 * 128;
constexpr unsigned ACC0 = 0u - 24u * ((1u << 26) + (1u << 19));
constexpr unsigned SPIN_CAP = 1u << 20;
constexpr int OOB = 0x40000000;

#define SB() __builtin_amdgcn_sched_barrier(0)

__device__ __forceinline__ rsrc_t mk(const void* p, int bytes) { return __builtin_amdgcn_make_buffer_rsrc((void*)p, 0, bytes, 0x00020000); }
template <int AUX> __device__ __forceinline__ u32x4 ld16(rsrc_t r, int voff, int soff) { return __builtin_amdgcn_raw_buffer_load_b128(r, voff, soff, AUX); }
__device__ __forceinline__ void st_unit(rsrc_t r, int voff, float a, float b, unsigned tag) {
  __builtin_amdgcn_raw_buffer_store_b128(u32x4{__float_as_uint(a), tag, __float_as_uint(b), tag}, r, voff, 0, AUX_SC1);
}
template <int N> __device__ __forceinline__ bool missing(const u32x4* a, unsigned t) {
  unsigned r = __builtin_amdgcn_bitop3_b32(a[0][1], a[0][3], t, 0x7e);
#pragma unroll
  for (int i = 1; i < N; ++i) r |= __builtin_amdgcn_bitop3_b32(a[i][1], a[i][3], t, 0x7e);
  return r != 0;
}
__device__ __forceinline__ float dot2(unsigned a, unsigned b, float c) {
  return __builtin_amdgcn_fdot2_f32_bf16(__builtin_bit_cast(bf16x2, a), __builtin_bit_cast(bf16x2, b), c, false);
}
__device__ __forceinline__ unsigned perm_lo(unsigned a, unsigned b) { return __builtin_amdgcn_perm(b, a, 0x05040100u); }
__device__ __forceinline__ unsigned perm_hi(unsigned a, unsigned b) { return __builtin_amdgcn_perm(b, a, 0x07060302u); }
__device__ __forceinline__ unsigned pack_bf16(float lo, float hi) { bf16x2 r = {(__bf16)lo, (__bf16)hi}; return __builtin_bit_cast(unsigned, r); }
__device__ __forceinline__ float f(unsigned u) { return __uint_as_float(u); }
template <int CTRL> __device__ __forceinline__ float dpp(float x) {
  return __builtin_bit_cast(float, __builtin_amdgcn_mov_dpp(__builtin_bit_cast(int, x), CTRL, 0xf, 0xf, true));
}
constexpr int XOR1 = 0xB1, XOR2 = 0x4E, XOR7 = 0x141, XOR8 = 0x128;
__device__ __forceinline__ float tr32(float a, float b) { auto s = __builtin_amdgcn_permlane32_swap(__float_as_uint(a), __float_as_uint(b), false, false); return f(s[0]) + f(s[1]); }
__device__ __forceinline__ float tr16(float a, float b) { auto s = __builtin_amdgcn_permlane16_swap(__float_as_uint(a), __float_as_uint(b), false, false); return f(s[0]) + f(s[1]); }
__device__ __forceinline__ float max32(float a) { auto s = __builtin_amdgcn_permlane32_swap(__float_as_uint(a), __float_as_uint(a), false, false); return fmaxf(f(s[0]), f(s[1])); }
__device__ __forceinline__ float max16(float a) { auto s = __builtin_amdgcn_permlane16_swap(__float_as_uint(a), __float_as_uint(a), false, false); return fmaxf(f(s[0]), f(s[1])); }
__device__ __forceinline__ float add8(float a) { return a + dpp<XOR8>(a); }
__device__ __forceinline__ float sum_r8(float a) { a = add8(a); a = tr16(a, a); return tr32(a, a); }
__device__ __forceinline__ float max_r8(float a) { a = fmaxf(a, dpp<XOR8>(a)); a = max16(a); return max32(a); }
#define LDS_WAIT() asm volatile("s_waitcnt lgkmcnt(0)" ::: "memory")
#define LDS_BARRIER() do { LDS_WAIT(); __builtin_amdgcn_s_barrier(); asm volatile("" ::: "memory"); SB(); } while (0)
__device__ __forceinline__ void pair8(float* acc, const u32x4& A, const u32x4& B, unsigned P) {
#pragma unroll
  for (int j = 0; j < 4; ++j) { acc[2 * j] = dot2(perm_lo(A[j], B[j]), P, acc[2 * j]); acc[2 * j + 1] = dot2(perm_hi(A[j], B[j]), P, acc[2 * j + 1]); }
}

struct __attribute__((aligned(16))) Smem {
  float att[8][200];
  float y[8][72];
  float pr[4][64];
  float hb[64];
  unsigned qb[96];
  unsigned short zb[8][32];
  unsigned qf[4];
  unsigned pf[4];
  float hs[4];
  float ml[16];
};
#define VMWAIT(n) do { SB(); asm volatile("s_waitcnt vmcnt(%0)" :: "n"(n) : "memory"); SB(); } while (0)
#define PACE(x_) do { asm volatile("1:\n\ts_memrealtime vcc\n\ts_waitcnt lgkmcnt(0)\n\ts_sub_i32 vcc_lo, vcc_lo, %0\n\ts_cmpk_lt_i32 vcc_lo, %1\n\ts_cbranch_scc1 1b" :: "s"(t0), "n"(x_) : "vcc", "scc", "memory"); SB(); } while (0)
#define LDS_POLL(cond) _Pragma("unroll 1") for (unsigned spins_ = 0; spins_ < SPIN_CAP; ++spins_) { if (cond) break; __builtin_amdgcn_s_sleep(1); } asm volatile("" ::: "memory")
template <int STRIDE> __device__ __forceinline__ float merge32(const LAS float* p) {
  const float a0 = p[0], a1 = p[16], b0 = p[STRIDE], b1 = p[STRIDE + 16];
  const float t = tr32(a0 + b0, a1 + b1);
  return tr16(t, t);
}
__device__ __forceinline__ void st_gran(rsrc_t r, int voff, float a, unsigned tag) { __builtin_amdgcn_raw_buffer_store_b64(u32x2{__float_as_uint(a), tag}, r, voff, 0, AUX_SC1); }
constexpr int P_PR = WIN, P_PUB = 9, P_G = 15, P_GP = P_G + 6, P_Q = P_GP + 3, P_SC = P_Q + 3, P_F = 27, P_FP = P_F + 12, P_ST = 33, P_SL = 42, P_SP = P_SL + 6;
static_assert(P_GP <= P_F && P_FP <= P_SL && P_SP <= 48 && P_ST > P_SC && P_SC >= 27 && P_SC <= 33, "order");
constexpr int sched(int p) { return TOFF + p / BU * BU * DT; }
constexpr int TLAST = sched(47);
constexpr int scpos(int g) { return g < 4 ? P_Q : P_SC; }
constexpr int inw(int e, int p) { return e > p - WMAX && e <= p; }
constexpr int extras(int p) { return inw(P_PUB, p) + 2 * inw(P_G, p) + 2 * inw(P_F, p) + 2 * inw(P_F + 3, p) + inw(P_F + 6, p) + inw(P_ST, p) + inw(P_SL, p); }

constexpr int ZNA[3] = {2, 3, 3}, ZNB[3] = {2, 3, 5}, ZTA[3] = {800, 1000, 840}, ZTB[3] = {0, 0, 1200}, ZTC[3] = {1450, 0, 0}, ZTG[3] = {2200, 2400, 2500};
template <int N> struct IC { static constexpr int value = N; };
template <int I, int N, class Fn> __device__ __forceinline__ void static_for(Fn&& fn) { if constexpr (I < N) { fn(IC<I>{}); static_for<I + 1, N>(fn); } }

__global__ void __launch_bounds__(512, 2) fused_kernel(const unsigned short* __restrict__ x, const unsigned short* __restrict__ wq, const unsigned short* __restrict__ wg,
                                                      const unsigned short* __restrict__ wu, const unsigned short* __restrict__ kc, unsigned char* ws, const unsigned short* __restrict__ vc,
                                                      const unsigned short* __restrict__ wo, const unsigned short* __restrict__ wd, const unsigned short* __restrict__ gw,
                                                      unsigned short* __restrict__ out) {
  __shared__ Smem sm;
  const int tid = threadIdx.x, lane = tid & 63, w = __builtin_amdgcn_readfirstlane(tid >> 6);
  const unsigned long long ts64 = __builtin_amdgcn_s_memrealtime();
  unsigned long long pc; int clen;
  asm volatile("s_getpc_b64 %0\n.Lcode_begin:\n\ts_mov_b32 %1, .Lcode_end-.Lcode_begin" : "=s"(pc), "=s"(clen));
  const unsigned cpf = __builtin_amdgcn_raw_buffer_load_b32(mk((const void*)pc, blockIdx.x < PFWG ? clen : 0), tid * 64, 0, 0);
  const int b = blockIdx.x, x7 = b & 7, j = b >> 3, h = x7 * 4 + (j >> 3), s = j & 7;
  const int j10 = (j * 26) >> 8, c = j - 10 * j10, g3 = j10, px = x7 ^ 4, G = 3 * px + g3;
  const bool proj = j < 30;
  const int r8 = lane >> 3, c8 = lane & 7, b4 = (lane >> 4) & 1, b5 = lane >> 5;
  const bool isq = w < 6, first = w < 3 || w == 6;
  const int w3 = w < 3 ? w : w - 3, c5 = c < 5 ? c : c - 5;
  const int nr = isq ? (w < 3 ? 6 : 4) : 8;
  int stride = isq ? H * DH * 2 : F * 2;
  const int rq = 80 * s + (w < 3 ? 32 : 0), rf = 128 * c5 + 64 * w - 384, cq = h * (DH * 2) + w3 * 128, cf = 128 * G;
  const int prow = (isq ? rq : rf) + r8 * nr;
  int pv = prow * stride + (isq ? cq : cf) + c8 * 16, xv = prow * 2, psz = isq ? D * H * DH * 2 : proj ? D * F * 2 : 0;
  int kvv = (h * SK + (SK - W) + s * MS + w * MW + r8) * (DH * 2) + c8 * 16;
  asm volatile("" : "+v"(pv), "+v"(xv), "+v"(kvv), "+s"(stride), "+s"(psz));
  SB();
  asm volatile("" :: "s"(x), "s"(kc), "s"(vc), "s"(wq), "s"(wo), "s"(wg), "s"(wu), "s"(wd), "s"(gw), "s"(out), "s"(ws), "s"(ts64));
  const rsrc_t rP = mk(isq ? wq : c < 5 ? wg : wu, psz), rW = mk(ws, WS_END), rX = mk(x, D * 2);
  const rsrc_t rK = mk(kc, H * SK * DH * 2), rV = mk(vc, H * SK * DH * 2);
  const unsigned ts = (unsigned)ts64;
  u32x4 wr[8];
#pragma unroll
  for (int i = 0; i < 4; ++i) wr[i] = ld16<AUX_NT>(rP, pv, i * stride);
  const u32x4 xa = ld16<0>(rX, xv, 0);
  if (nr > 4) {
#pragma unroll
    for (int i = 4; i < 6; ++i) wr[i] = ld16<AUX_NT>(rP, pv, i * stride);
    if (nr > 6) {
#pragma unroll
      for (int i = 6; i < 8; ++i) wr[i] = ld16<AUX_NT>(rP, pv, i * stride);
    }
  }
  const int rc = (j - 30) * 8 + x7;
  if (w == 6 && !proj) __builtin_amdgcn_raw_buffer_store_b64(u32x2{0u, ACC0}, rW, rc < 10 ? WS_ACC + (rc * 8 + r8) * ASTR + c8 * 8 : OOB, 0, AUX_SC1);
  SB();
  if (lane < 28) sm.qf[lane] = 0;
  LDS_BARRIER();
  const unsigned hdr = __builtin_amdgcn_raw_buffer_load_b32(rW, 0, 0, AUX_SC1);
  SB();
  u32x4 k[24], v[24], ex[5];
  unsigned tag, t0 = ts + ((h & 3) * (BU * DT / 40)), pp[4], q[12];
  float p0, p1, m, gate, sc[8], acc[24] = {};
  static_for<0, 49>([&](auto ic) __attribute__((always_inline)) {
    constexpr int p = decltype(ic)::value;
    if constexpr (p == P_PR) {
      tag = __builtin_amdgcn_readfirstlane(hdr) + 1u;
      asm volatile("" :: "v"(cpf));
      float a[8] = {};
      pair8(a, wr[0], wr[1], xa[0]); pair8(a, wr[2], wr[3], xa[1]);
      if (nr > 4) { pair8(a, wr[4], wr[5], xa[2]); if (nr > 6) pair8(a, wr[6], wr[7], xa[3]); }
      float t[4];
#pragma unroll
      for (int i = 0; i < 4; ++i) t[i] = tr32(a[i], a[i + 4]);
      p0 = add8(tr16(t[0], t[2])); p1 = add8(tr16(t[1], t[3]));
      if (!first) {
        const int sl = w3 < 3 ? w3 : 3;
        *(f32x2*)&sm.pr[sl][c8 * 8 + b5 * 4 + b4 * 2] = f32x2{p0, p1};
        asm volatile("" ::: "memory");
        if (lane == 0) *(volatile LAS unsigned*)&sm.pf[sl] = 1u;
      }
    }
    if constexpr (p == P_PUB) if (first) {
      LDS_POLL(__builtin_amdgcn_readfirstlane(*(volatile LAS unsigned*)&sm.pf[w3]));
      const f32x2 t = *(const f32x2*)&sm.pr[w3][c8 * 8 + b5 * 4 + b4 * 2];
      st_unit(rW, isq || proj ? (!(lane & 8) ? (isq ? WS_R0 + b * REC0 + w * 512 : WS_RF + (G * 10 + c) * RECF) + (c8 * 4 + b5 * 2 + b4) * 16 : OOB) : lane == 0 && rc < 10 ? WS_FL + rc * 128 : OOB, p0 + t[0], p1 + t[1], tag);
    }
    const int qo = WS_R0 + ((b & ~56) + r8 * 8) * REC0 + (w * 16 + c8) * 16, ho = WS_RF + (G * 10 + 5 * b5) * RECF + (lane & 31) * 16;
    if constexpr (p == P_G) if (w < 6) { ex[0] = ld16<AUX_SC1>(rW, qo, 0); ex[1] = ld16<AUX_SC1>(rW, qo, 128); }
    if constexpr (p == P_GP) if (w < 6) {
      if (__builtin_expect(__any(missing<2>(ex, tag)), 0)) {
#pragma unroll 1
        for (unsigned spins = 0; spins < SPIN_CAP; ++spins) {
          asm volatile("" ::: "memory");
#pragma unroll
          for (int i = 0; i < 2; ++i) ex[i] = ld16<AUX_SC1>(rW, qo, i * 128);
          if (!__any(missing<2>(ex, tag))) break;
        }
      }
      const float t = add8(tr16(tr32(f(ex[0][0]), f(ex[1][0])), tr32(f(ex[0][2]), f(ex[1][2]))));
      ((unsigned short*)sm.qb)[32 * w + 16 * b5 + 2 * c8 + b4] = (unsigned short)pack_bf16(t, t);
      asm volatile("" ::: "memory");
      if (lane == 0) __hip_atomic_fetch_add((LAS unsigned*)sm.qf, 1u, __ATOMIC_RELAXED, __HIP_MEMORY_SCOPE_WORKGROUP);
    }
    if constexpr (p >= P_F && p <= P_F + 6 && (p - P_F) % 3 == 0) if (w < 2) {
#pragma unroll
      for (int i = (p - P_F) / 3 * 2; i < (p - P_F) / 3 * 2 + 2 && i < 5; ++i) {
        const bool rec = w == 0 || i == 4;
        ex[i] = ld16<AUX_SC1>(mk(rec ? ws : (const void*)(i & 1 ? x : gw), rec ? WS_END : D * 2), w == 0 ? (proj ? ho : OOB) + i * RECF : i == 4 ? WS_FL + c * 128 : lane * 16 + (i >> 1) * 1024, 0);
      }
    }
    if constexpr (p == P_FP) {
      if (w == 0 && proj) {
        if (__builtin_expect(__any(missing<5>(ex, tag)), 0)) {
#pragma unroll 1
          for (unsigned spins = 0; spins < SPIN_CAP; ++spins) {
            asm volatile("" ::: "memory");
#pragma unroll
            for (int i = 0; i < 5; ++i) ex[i] = ld16<AUX_SC1>(rW, ho, i * RECF);
            if (!__any(missing<5>(ex, tag))) break;
          }
        }
        const float s0 = (f(ex[0][0]) + f(ex[1][0])) + (f(ex[2][0]) + f(ex[3][0])) + f(ex[4][0]), s1 = (f(ex[0][2]) + f(ex[1][2])) + (f(ex[2][2]) + f(ex[3][2])) + f(ex[4][2]);
        const auto t = __builtin_amdgcn_permlane32_swap(__float_as_uint(s0), __float_as_uint(s1), false, false);
        const float a = f(t[0]), u = f(t[1]);
        const float z = 0.7978845608028654f * (a + 0.044715f * a * a * a);
        sm.hb[(lane & 31) * 2 + b5] = 0.5f * a * (2.f - 2.f / (1.f + __expf(2.f * z))) * u;
      } else if (w == 1) {
        float t = 0.f;
#pragma unroll
        for (int i = 0; i < 4; ++i) { t = dot2(ex[0][i], ex[1][i], t); t = dot2(ex[2][i], ex[3][i], t); }
        t += dpp<XOR1>(t); t += dpp<XOR2>(t); t += dpp<XOR7>(t); t = sum_r8(t);
        gate = 1.f / (1.f + __expf(-t));
        if (__builtin_expect(__any(missing<1>(ex + 4, tag)), 0)) {
#pragma unroll 1
          for (unsigned spins = 0; spins < SPIN_CAP; ++spins) {
            asm volatile("" ::: "memory");
            ex[4] = ld16<AUX_SC1>(rW, WS_FL + c * 128, 0);
            if (!__any(missing<1>(ex + 4, tag))) break;
          }
        }
      }
    }
    if constexpr (p == P_Q) {
      LDS_POLL(__builtin_amdgcn_readfirstlane(*(volatile LAS unsigned*)sm.qf) == 6);
#pragma unroll
      for (int i = 0; i < 3; ++i) { const u32x4 t = *(const u32x4*)&sm.qb[i * 32 + c8 * 4]; q[i * 4] = t[0]; q[i * 4 + 1] = t[1]; q[i * 4 + 2] = t[2]; q[i * 4 + 3] = t[3]; }
    }
#pragma unroll
    for (int g = 0; g < 8; ++g)
      if (p == scpos(g)) {
        float a0 = 0.f, a1 = 0.f;
#pragma unroll
        for (int i = 0; i < 3; ++i) { a0 = dot2(k[g * 3 + i][0], q[i * 4], a0); a1 = dot2(k[g * 3 + i][1], q[i * 4 + 1], a1); a0 = dot2(k[g * 3 + i][2], q[i * 4 + 2], a0); a1 = dot2(k[g * 3 + i][3], q[i * 4 + 3], a1); }
        a0 += a1; a0 += dpp<XOR1>(a0); a0 += dpp<XOR2>(a0); a0 += dpp<XOR7>(a0);
        sc[g] = f(pack_bf16(0.f, f(pack_bf16(0.f, a0)) * 0.07216878364870322f)) * 1.4426950408889634f;
        asm volatile("" : "+v"(sc[g]));
      }
    if constexpr (p == P_SC) {
      m = max_r8(fmaxf(fmaxf(fmaxf(sc[0], sc[1]), fmaxf(sc[2], sc[3])), fmaxf(fmaxf(sc[4], sc[5]), fmaxf(sc[6], sc[7]))));
      float lsum = 0.f;
#pragma unroll
      for (int g = 0; g < 4; ++g) {
        const float e0 = __builtin_amdgcn_exp2f(sc[2 * g] - m), e1 = __builtin_amdgcn_exp2f(sc[2 * g + 1] - m);
        lsum += e0 + e1; pp[g] = pack_bf16(e0, e1);
        asm volatile("" : "+v"(pp[g]));
      }
      lsum = sum_r8(lsum);
      if (lane == 0) *(volatile LAS u32x2*)&sm.ml[2 * w] = u32x2{__float_as_uint(m), __float_as_uint(lsum)};
    }
    const int so = WS_R1 + ((b & ~56) + c8 * 8) * REC1 + 96 * 16;
    if constexpr (p == P_ST) if (w == 1) {
      u32x2 t;
      LDS_POLL((t = *(volatile LAS u32x2*)&sm.ml[2 * c8], !__any(t[1] == 0)));
      float M = f(t[0]);
      M = fmaxf(M, dpp<XOR1>(M)); M = fmaxf(M, dpp<XOR2>(M)); M = fmaxf(M, dpp<XOR7>(M));
      float L = __builtin_amdgcn_exp2f(f(t[0]) - M) * f(t[1]);
      L += dpp<XOR1>(L); L += dpp<XOR2>(L); L += dpp<XOR7>(L);
      st_unit(rW, lane == 0 ? WS_R1 + b * REC1 + 96 * 16 : OOB, M, L, tag);
    }
    if constexpr (p == P_SL) if (w == 1) ex[0] = ld16<AUX_SC1>(rW, lane < 8 ? so : OOB, 0);
    if constexpr (p == P_SP) if (w == 1) {
      if (__builtin_expect(__any(lane < 8 && missing<1>(ex, tag)), 0)) {
#pragma unroll 1
        for (unsigned spins = 0; spins < SPIN_CAP; ++spins) {
          asm volatile("" ::: "memory");
          ex[0] = ld16<AUX_SC1>(rW, lane < 8 ? so : OOB, 0);
          if (!__any(lane < 8 && missing<1>(ex, tag))) break;
        }
      }
      float M = f(ex[0][0]);
      M = fmaxf(M, dpp<XOR1>(M)); M = fmaxf(M, dpp<XOR2>(M)); M = fmaxf(M, dpp<XOR7>(M));
      float L = __builtin_amdgcn_exp2f(f(ex[0][0]) - M) * f(ex[0][2]);
      L += dpp<XOR1>(L); L += dpp<XOR2>(L); L += dpp<XOR7>(L);
      if (lane == 0) *(volatile LAS u32x4*)sm.hs = u32x4{__float_as_uint(M), __float_as_uint(gate * __builtin_amdgcn_rcpf(L)), __float_as_uint(gate), 1u};
    }
    if constexpr (p >= 33 && p % 3 == 0) {
      constexpr int n = (p - 33) / 6;
#pragma unroll
      for (int i = (p - 33) % 6 ? 2 : 0; i < ((p - 33) % 6 ? 3 : 2); ++i) {
        pair8(acc + i * 8, v[6 * n + i], v[6 * n + 3 + i], pp[n]);
#pragma unroll
        for (int e = 0; e < 8; ++e) asm volatile("" : "+v"(acc[i * 8 + e]));
      }
    }
    SB();
    if constexpr (p >= WIN && p < 48) { constexpr int nw = (p < PP0 ? WIN : WMAX) - 1 + extras(p); VMWAIT(nw); }
    if constexpr (p >= PP0 && p < 48 && p % BU == 0) PACE(sched(p) / 10);
    if constexpr (p < 24) k[p] = ld16<AUX_NT>(rK, kvv + (p % 3) * 128, ((p / 3 + h) & 7) * (MG * 384));
    else if constexpr (p < 48) v[p - 24] = ld16<AUX_NT>(rV, kvv + (p % 3) * 128, ((p / 3 + h) & 7) * (MG * 384));
    SB();
  });
  const rsrc_t rO = mk(wo, proj ? H * DH * D * 2 : 0), rD = mk(wd, proj ? F * D * 2 : 0);
  int ov = (256 * G + 32 * w + 4 * r8) * (D * 2) + 128 * c + c8 * 16, dv = (64 * G + 8 * w + r8) * (D * 2) + 128 * c + c8 * 16;
  u32x4 hs, a[2];
  unsigned zp[2][8];
  float y[8];
#define LDZ(n0, n1) _Pragma("unroll") for (int i_ = n0; i_ < n1; ++i_) z[i_] = i_ < 4 ? ld16<AUX_NT>(rO, ov, i_ * D * 2) : ld16<AUX_NT>(rD, dv, 0); SB()
  float scale;
  const int l4 = lane & 15, lr = 256 * g3 + 32 * w, hl = (lr * 43) >> 13;
  int mo = w < 6 && !b4 ? WS_R1 + b * REC1 + (32 * w + 16 * b5 + l4) * 8 : OOB;
  const LAS float* ma = (const LAS float*)&sm.att[2 * (lane >> 4)][(w < 6 ? 32 * w : 0) + l4];
  LAS f32x2* aw = (LAS f32x2*)&sm.att[w][c8 * 8 + b5 * 4 + b4 * 2];
  int ro = WS_R1 + (px + 8 * (8 * hl + r8)) * REC1 + (lr - 192 * hl) * 8 + c8 * 16;
  asm volatile("" : "+v"(mo), "+v"(ma), "+v"(aw), "+v"(ro), "+v"(ov), "+v"(dv));
  t0 = ts;
  auto tail = [&](auto ig) __attribute__((always_inline)) {
  constexpr int gi = decltype(ig)::value, NA = ZNA[gi], NB = ZNB[gi];
  u32x4 z[5];
  if (NA > 0) { PACE((TLAST + ZTA[gi]) / 10); LDZ(0, NA); }
  if (NB > NA) { PACE((TLAST + ZTB[gi]) / 10); LDZ(NA, NB); }
#pragma unroll
  for (int i = 0; i < 3; ++i) {
    pair8(acc + i * 8, v[18 + i], v[21 + i], pp[3]);
    float t[4];
#pragma unroll
    for (int e = 0; e < 4; ++e) t[e] = tr32(acc[i * 8 + e], acc[i * 8 + e + 4]);
    f32x2 r = {add8(tr16(t[0], t[2])), add8(tr16(t[1], t[3]))};
    if (i == 0) {
      asm volatile("" : "+v"(r));
      LDS_POLL((hs = *(volatile LAS u32x4*)sm.hs, __builtin_amdgcn_readfirstlane(hs[3])));
      scale = __builtin_amdgcn_exp2f(m - f(hs[0])) * f(hs[1]);
    }
    aw[32 * i] = r * scale;
    SB();
  }
  LDS_BARRIER();
  if (w < 6) st_gran(rW, mo, merge32<200>(ma), tag);
  SB();
  if (proj) {
    if (NB < 5) { PACE((TLAST + ZTC[gi]) / 10); LDZ(NB, 5); }
    PACE((TLAST + ZTG[gi]) / 10);
#pragma unroll
    for (int i = 0; i < 2; ++i) a[i] = ld16<AUX_SC1>(rW, ro, i * 128);
    SB();
#pragma unroll
    for (int i = 0; i < 2; ++i)
#pragma unroll
      for (int e = 0; e < 4; ++e) { zp[i][2 * e] = perm_lo(z[2 * i][e], z[2 * i + 1][e]); zp[i][2 * e + 1] = perm_hi(z[2 * i][e], z[2 * i + 1][e]); asm volatile("" : "+v"(zp[i][2 * e]), "+v"(zp[i][2 * e + 1])); }
    const float hv = sm.hb[8 * w + r8] * (1.f - f(hs[2]));
    const unsigned h0 = pack_bf16(hv, 0.f), h1 = h0 << 16;
#pragma unroll
    for (int e = 0; e < 4; ++e) { y[2 * e] = dot2(z[4][e], h0, 0.f); y[2 * e + 1] = dot2(z[4][e], h1, 0.f); asm volatile("" : "+v"(y[2 * e]), "+v"(y[2 * e + 1])); }
  }
  };
  if (j10 == 0) tail(IC<0>{}); else if (j10 == 1) tail(IC<1>{}); else tail(IC<2>{});
  if (proj) {
    LAS unsigned short* zw = (LAS unsigned short*)&sm.zb[w][16 * b5 + 2 * c8 + b4];
    const LAS u32x2* zr = (const LAS u32x2*)&sm.zb[w][4 * r8];
    LAS f32x2* yw = (LAS f32x2*)&sm.y[w][c8 * 8 + b5 * 4 + b4 * 2];
    const LAS float* yr = (const LAS float*)&sm.y[2 * (lane >> 4)][32 * (w & 1) + l4];
    int yo = !b4 ? WS_R2 + (c * 24 + G) * REC2 + (32 * w + 16 * b5 + l4) * 8 : OOB;
    const int col = 64 * c + 32 * (w & 1) + 16 * b5 + l4;
    int oo = col * 2;
    GAS unsigned long long* ap = (GAS unsigned long long*)(ws + WS_ACC + (col >> 3) * ASTR + (col & 7) * 8);
    const rsrc_t rY = mk(out, D * 2);
    asm volatile("" : "+v"(zw), "+v"(zr), "+v"(yw), "+v"(yr), "+v"(yo), "+v"(oo), "+v"(ap));
    SB();
#pragma unroll 1
    for (unsigned spins = 0; spins < SPIN_CAP && missing<2>(a, tag); ++spins) {
      asm volatile("" ::: "memory");
#pragma unroll
      for (int i = 0; i < 2; ++i) a[i] = ld16<AUX_SC1>(rW, ro, i * 128);
    }
    {
      const float t = add8(tr16(tr32(f(a[0][0]), f(a[1][0])), tr32(f(a[0][2]), f(a[1][2]))));
      *zw = (unsigned short)pack_bf16(t, t);
    }
    asm volatile("" ::: "memory");
    {
      const u32x2 za = *zr;
#pragma unroll
      for (int i = 0; i < 2; ++i)
#pragma unroll
        for (int e = 0; e < 8; ++e) y[e] = dot2(zp[i][e], za[i], y[e]);
      float t[4];
#pragma unroll
      for (int e = 0; e < 4; ++e) t[e] = tr32(y[e], y[e + 4]);
      *yw = f32x2{add8(tr16(t[0], t[2])), add8(tr16(t[1], t[3]))};
    }
    LDS_BARRIER();
    if (w < 2) {
      const float yv = merge32<72>(yr);
      const u32x2 d = __builtin_bit_cast(u32x2, (double)yv + 192.0);
      const unsigned long long mine = __builtin_bit_cast(unsigned long long, u32x2{d[0], (d[1] & 0xfffffu) | (1u << 26)});
      const bool inr = fabsf(yv) < 64.f;
      unsigned long long old = 1ull << 63;
      if (!b4 && inr) old = __hip_atomic_fetch_add(ap, mine, __ATOMIC_RELAXED, __HIP_MEMORY_SCOPE_AGENT);
      SB();
      st_gran(rW, yo, yv, tag);
      SB();
      const u32x2 t = __builtin_bit_cast(u32x2, old + mine);
      float r = (float)__builtin_fma((double)(int)t[1], 0x1p32, (double)t[0]) * 0x1p-45f;
      const bool last = (int)t[1] > -(1 << 25);
      __builtin_amdgcn_raw_buffer_store_b16((unsigned short)pack_bf16(r, r), rY, last ? oo : OOB, 0, AUX_SC1);
      SB();
      const bool slow = !b4 && (!inr || (last && fabsf(r) < 0x1p-23f));
      if (__builtin_expect(slow, 0)) {
        const int go = WS_R2 + c * 24 * REC2 + (32 * w + 16 * b5 + l4) * 8;
#pragma unroll 1
        for (unsigned spins = 0; spins < SPIN_CAP; ++spins) {
          asm volatile("" ::: "memory");
          u32x2 e[24];
#pragma unroll
          for (int i = 0; i < 24; ++i) e[i] = __builtin_amdgcn_raw_buffer_load_b64(rW, go, i * REC2, AUX_SC1);
          unsigned miss = 0; r = 0.f;
#pragma unroll
          for (int i = 0; i < 24; ++i) { miss |= e[i][1] ^ tag; r += f(e[i][0]); }
          if (!miss) break;
        }
        __builtin_amdgcn_raw_buffer_store_b16((unsigned short)pack_bf16(r, r), rY, oo, 0, AUX_SC1);
      }
      asm volatile(".Lcode_end:");
      if ((last || slow) && oo == 0) __builtin_amdgcn_raw_buffer_store_b32(tag, rW, 0, 0, AUX_SC1);
    }
  }
}

extern "C" void kernel_launch(void* const* d_in, const int* in_sizes, int n_in, void* d_out, int out_size, void* d_ws, size_t ws_size, hipStream_t stream) {
  fused_kernel<<<256, 512, 0, stream>>>((const unsigned short*)d_in[0], (const unsigned short*)d_in[3], (const unsigned short*)d_in[6], (const unsigned short*)d_in[7],
                                        (const unsigned short*)d_in[1], (unsigned char*)d_ws, (const unsigned short*)d_in[2], (const unsigned short*)d_in[4],
                                        (const unsigned short*)d_in[8], (const unsigned short*)d_in[9], (unsigned short*)d_out);
}
```
